```python
import torch

def _softmax(x, axis=-1):
    return torch.softmax(x, dim=axis)

def _gelu(x):
    return torch.nn.functional.gelu(x, approximate='tanh')

def _silu(x):
    return torch.nn.functional.silu(x)

def _sigmoid(x):
    return torch.sigmoid(x)

def _layernorm(x, axis=-1, eps=1e-05):
    m = x.mean(dim=axis, keepdim=True)
    v = x.var(dim=axis, keepdim=True, unbiased=False)
    return (x - m) / torch.sqrt(v + eps)

def _scatter_add(tgt, idx, src, axis=0):
    return tgt.clone().index_add_(axis, idx, src)

def _segment_sum(src, seg, n):
    out = torch.zeros((n,) + src.shape[1:], dtype=src.dtype)
    return out.index_add_(0, seg, src)

def _pad(x, pw):
    flat = tuple((v for pair in reversed(pw) for v in pair))
    return torch.nn.functional.pad(x, flat)
kernel_name = 'standalone__pixart__diffusion'

def setup_inputs():

    _w = lambda a: (lambda t: t.to(torch.bfloat16) if t.is_floating_point() else t)(torch.as_tensor(a))
    B, N, D, M = (2, 1024, 1152, 77)
    return {'x': _w((lambda *s: torch.randn(*s, dtype=torch.bfloat16) * 0.05)(B, N, D)), 'c': _w((lambda *s: torch.randn(*s, dtype=torch.bfloat16) * 0.05)(B, D)), 'y': _w((lambda *s: torch.randn(*s, dtype=torch.bfloat16) * 0.1)(B, M, D)), 'wqkv': _w((lambda *s: torch.randn(*s, dtype=torch.bfloat16) * 0.05)(D, 3 * D)), 'wo': _w((lambda *s: torch.randn(*s, dtype=torch.bfloat16) * 0.05)(D, D)), 'wq_c': _w((lambda *s: torch.randn(*s, dtype=torch.bfloat16) * 0.1)(D, D)), 'wkv_c': _w((lambda *s: torch.randn(*s, dtype=torch.bfloat16) * 0.1)(D, 2 * D)), 'wo_c': _w((lambda *s: torch.randn(*s, dtype=torch.bfloat16) * 0.05)(D, D)), 'w_mod': _w((lambda *s: torch.randn(*s, dtype=torch.bfloat16) * 0.05)(D, 6 * D))}

def reference(x, c, y, wqkv, wo, wq_c, wkv_c, wo_c, w_mod):
    B, N, D = x.shape
    mod = (_silu(c) @ w_mod).reshape(B, 6, 1, D)
    shift_a, scale_a, gate_a, shift_m, scale_m, gate_m = [mod[:, i] for i in range(6)]
    h = _layernorm(x, axis=-1) * (1 + scale_a) + shift_a
    qkv = h @ wqkv
    q, k, v = (qkv[..., :D].clone(), qkv[..., D:2 * D].clone(), qkv[..., 2 * D:].clone())
    p = _softmax(torch.einsum('bqd,bkd->bqk', q, k) * D ** (-0.5), axis=-1)
    x = x + gate_a * (torch.einsum('bqk,bkd->bqd', p, v) @ wo)
    hc = _layernorm(x, axis=-1)
    qc = hc @ wq_c
    kvc = y @ wkv_c
    kc, vc = (kvc[..., :D].clone(), kvc[..., D:].clone())
    pc = _softmax(torch.einsum('bqd,bkd->bqk', qc, kc) * D ** (-0.5), axis=-1)
    return x + torch.einsum('bqk,bkd->bqd', pc, vc) @ wo_c
```

```cpp
#include <hip/hip_runtime.h>
#include <hip/hip_bf16.h>
#include <stdint.h>
typedef __hip_bfloat16 bf16;
constexpr int B_=2, N_=1024, D_=1152, M_=77;

__device__ __forceinline__ float bf2f(unsigned short v){ return __uint_as_float(((unsigned)v)<<16); }
__device__ __forceinline__ unsigned short f2bf(float f){ __bf16 h = (__bf16)f; return __builtin_bit_cast(unsigned short,h); }

template<bool OUTF32>
__global__ __launch_bounds__(256) void gemm_simple(const unsigned short* A, long sam, long sak, long sab,
                            const unsigned short* Bm, long sbk, long sbn, long sbb,
                            void* C, long scm, long scb, int M, int N, int K) {
  __shared__ float As[32][65], Bs[32][65];
  int b = blockIdx.z;
  A += b*sab; Bm += b*sbb;
  int m0 = blockIdx.y*64, n0 = blockIdx.x*64;
  int tx = threadIdx.x & 15, ty = threadIdx.x >> 4;
  float acc[4][4] = {};
  for (int k0=0;k0<K;k0+=32){
    for (int i=threadIdx.x;i<64*32;i+=256){
      int kk = i & 31, mm = i >> 5;
      int m = m0+mm, k = k0+kk;
      As[kk][mm] = (m<M && k<K) ? bf2f(A[m*sam + k*sak]) : 0.f;
      int n = n0+mm;
      Bs[kk][mm] = (n<N && k<K) ? bf2f(Bm[k*sbk + n*sbn]) : 0.f;
    }
    __syncthreads();
    for (int kk=0;kk<32;kk++){
      float a[4], bb[4];
      for (int i=0;i<4;i++){ a[i]=As[kk][ty*4+i]; bb[i]=Bs[kk][tx*4+i]; }
      for (int i=0;i<4;i++) for(int j=0;j<4;j++) acc[i][j] += a[i]*bb[j];
    }
    __syncthreads();
  }
  for (int i=0;i<4;i++) for(int j=0;j<4;j++){
    int m=m0+ty*4+i, n=n0+tx*4+j;
    if (m<M && n<N){
      if (OUTF32) ((float*)C)[b*scb + m*scm + n] = acc[i][j];
      else ((unsigned short*)C)[b*scb + m*scm + n] = f2bf(acc[i][j]);
    }
  }
}
__global__ void silu_k(const unsigned short* c, unsigned short* o, int n){
  int i = blockIdx.x*256+threadIdx.x; if(i<n){ float v=bf2f(c[i]); o[i]=f2bf(v/(1.f+__expf(-v))); }
}
__device__ float block_sum(float v, float* sh){
  for(int o=32;o;o>>=1) v += __shfl_xor(v,o);
  __syncthreads();
  if((threadIdx.x&63)==0) sh[threadIdx.x>>6]=v;
  __syncthreads();
  return sh[0]+sh[1]+sh[2]+sh[3];
}
__device__ float block_max(float v, float* sh){
  for(int o=32;o;o>>=1) v = fmaxf(v,__shfl_xor(v,o));
  __syncthreads();
  if((threadIdx.x&63)==0) sh[threadIdx.x>>6]=v;
  __syncthreads();
  return fmaxf(fmaxf(sh[0],sh[1]),fmaxf(sh[2],sh[3]));
}
__global__ void ln_k(const unsigned short* x, const unsigned short* mod, unsigned short* h){
  __shared__ float sh[4];
  int r = blockIdx.x, b = r / N_;
  float v[5]; float s=0;
  for(int i=0;i<5;i++){ int d=threadIdx.x+i*256; v[i]= d<D_? bf2f(x[(long)r*D_+d]):0.f; s+=v[i]; }
  float mu = block_sum(s,sh)/D_;
  float q=0; for(int i=0;i<5;i++){ int d=threadIdx.x+i*256; if(d<D_){ float t=v[i]-mu; q+=t*t; } }
  float var = block_sum(q,sh)/D_;
  float rs = 1.f/sqrtf(var+1e-5f);
  for(int i=0;i<5;i++){ int d=threadIdx.x+i*256; if(d<D_){ float t=(v[i]-mu)*rs;
     if(mod){ float sc=bf2f(mod[b*3*D_+D_+d]), sf=bf2f(mod[b*3*D_+d]); t = t*(1.f+sc)+sf; }
     h[(long)r*D_+d]=f2bf(t);} }
}
__global__ void softmax_k(const float* S, unsigned short* P, int n, int ld, float scale){
  __shared__ float sh[4];
  long r = blockIdx.x;
  float m=-INFINITY; for(int i=threadIdx.x;i<n;i+=256) m=fmaxf(m,S[r*ld+i]*scale);
  m=block_max(m,sh);
  float s=0; for(int i=threadIdx.x;i<n;i+=256) s+=__expf(S[r*ld+i]*scale-m);
  s=block_sum(s,sh);
  for(int i=threadIdx.x;i<n;i+=256) P[r*ld+i]=f2bf(__expf(S[r*ld+i]*scale-m)/s);
}
__global__ void resid_k(const unsigned short* x, const float* o, const unsigned short* mod, unsigned short* out){
  long i = blockIdx.x*256L+threadIdx.x; int d=i%D_; int b=i/((long)N_*D_);
  float g = mod? bf2f(mod[b*3*D_+2*D_+d]) : 1.f;
  out[i]=f2bf(bf2f(x[i])+g*o[i]);
}

extern "C" void kernel_launch(void* const* d_in, const int* in_sizes, int n_in,
                              void* d_out, int out_size, void* d_ws, size_t ws_size,
                              hipStream_t stream) {
  typedef unsigned short us;
  const us* x=(const us*)d_in[0]; const us* c=(const us*)d_in[1]; const us* y=(const us*)d_in[2];
  const us* wqkv=(const us*)d_in[3]; const us* wo=(const us*)d_in[4]; const us* wq_c=(const us*)d_in[5];
  const us* wkv_c=(const us*)d_in[6]; const us* wo_c=(const us*)d_in[7]; const us* w_mod=(const us*)d_in[8];
  char* ws=(char*)d_ws; size_t off=0;
  auto alloc=[&](size_t bytes){ void* p=ws+off; off+=(bytes+255)&~(size_t)255; return p; };
  us* sc=(us*)alloc(B_*D_*2); us* mod=(us*)alloc(B_*3*D_*2);
  us* h=(us*)alloc((size_t)B_*N_*D_*2); us* Wqk=(us*)alloc((size_t)D_*D_*2); us* Wvo=(us*)alloc((size_t)D_*D_*2);
  us* kvc=(us*)alloc((size_t)B_*M_*2*D_*2); us* T=(us*)alloc((size_t)B_*N_*D_*2); us* V2=(us*)alloc((size_t)B_*N_*D_*2);
  float* S=(float*)alloc((size_t)B_*N_*N_*4); us* P=(us*)alloc((size_t)B_*N_*N_*2);
  float* O=(float*)alloc((size_t)B_*N_*D_*4); us* x2=(us*)alloc((size_t)B_*N_*D_*2); us* hc=(us*)alloc((size_t)B_*N_*D_*2);
  us* Wk=(us*)alloc((size_t)B_*D_*80*2); us* Vp=(us*)alloc((size_t)B_*80*D_*2);
  float* Sc=(float*)alloc((size_t)B_*N_*80*4); us* Pc=(us*)alloc((size_t)B_*N_*80*2);
  const float scale = 1.0f/sqrtf((float)D_);
  auto g=[&](int M,int N,int nb){ return dim3((N+63)/64,(M+63)/64,nb); };
  silu_k<<<(B_*D_+255)/256,256,0,stream>>>(c,sc,B_*D_);
  gemm_simple<false><<<g(B_,3*D_,1),256,0,stream>>>(sc,D_,1,0,w_mod,6*D_,1,0,mod,3*D_,0,B_,3*D_,D_);
  ln_k<<<B_*N_,256,0,stream>>>(x,mod,h);
  gemm_simple<false><<<g(D_,D_,1),256,0,stream>>>(wqkv,3*D_,1,0,wqkv+D_,1,3*D_,0,Wqk,D_,0,D_,D_,D_);
  gemm_simple<false><<<g(D_,D_,1),256,0,stream>>>(wqkv+2*D_,3*D_,1,0,wo,D_,1,0,Wvo,D_,0,D_,D_,D_);
  gemm_simple<false><<<g(B_*M_,2*D_,1),256,0,stream>>>(y,D_,1,0,wkv_c,2*D_,1,0,kvc,2*D_,0,B_*M_,2*D_,D_);
  gemm_simple<false><<<g(B_*N_,D_,1),256,0,stream>>>(h,D_,1,0,Wqk,D_,1,0,T,D_,0,B_*N_,D_,D_);
  gemm_simple<false><<<g(B_*N_,D_,1),256,0,stream>>>(h,D_,1,0,Wvo,D_,1,0,V2,D_,0,B_*N_,D_,D_);
  gemm_simple<true><<<g(N_,N_,B_),256,0,stream>>>(T,D_,1,(long)N_*D_,h,1,D_,(long)N_*D_,S,N_,(long)N_*N_,N_,N_,D_);
  softmax_k<<<B_*N_,256,0,stream>>>(S,P,N_,N_,scale);
  gemm_simple<true><<<g(N_,D_,B_),256,0,stream>>>(P,N_,1,(long)N_*N_,V2,D_,1,(long)N_*D_,O,D_,(long)N_*D_,N_,D_,N_);
  resid_k<<<B_*N_*D_/256,256,0,stream>>>(x,O,mod,x2);
  ln_k<<<B_*N_,256,0,stream>>>(x2,nullptr,hc);
  gemm_simple<false><<<g(D_,M_,B_),256,0,stream>>>(wq_c,D_,1,0,kvc,1,2*D_,(long)M_*2*D_,Wk,80,(long)D_*80,D_,M_,D_);
  gemm_simple<false><<<g(M_,D_,B_),256,0,stream>>>(kvc+D_,2*D_,1,(long)M_*2*D_,wo_c,D_,1,0,Vp,D_,(long)80*D_,M_,D_,D_);
  gemm_simple<true><<<g(N_,M_,B_),256,0,stream>>>(hc,D_,1,(long)N_*D_,Wk,80,1,(long)D_*80,Sc,80,(long)N_*80,N_,M_,D_);
  softmax_k<<<B_*N_,256,0,stream>>>(Sc,Pc,M_,80,scale);
  gemm_simple<true><<<g(N_,D_,B_),256,0,stream>>>(Pc,80,1,(long)N_*80,Vp,D_,1,(long)80*D_,O,D_,(long)N_*D_,N_,D_,M_);
  resid_k<<<B_*N_*D_/256,256,0,stream>>>(x2,O,nullptr,(us*)d_out);
}
```

```cpp
#include <hip/hip_runtime.h>
#include <utility>
#include <stdint.h>

namespace {
typedef unsigned v4u __attribute__((ext_vector_type(4)));
typedef unsigned v2u __attribute__((ext_vector_type(2)));
typedef float f32x4 __attribute__((ext_vector_type(4)));
typedef float f32x2 __attribute__((ext_vector_type(2)));
typedef short bf16x8 __attribute__((ext_vector_type(8)));
typedef __bf16 bf16x2_t __attribute__((ext_vector_type(2)));
typedef __amdgpu_buffer_rsrc_t rsrc_t;
typedef unsigned short us;
#define GAS __attribute__((address_space(1)))
#define LAS __attribute__((address_space(3)))
#define DEV __device__ __forceinline__
#define SB() __builtin_amdgcn_sched_barrier(0)
#define IC(x) std::integral_constant<int, (x)>{}
#define CV(x) decltype(x)::value

template <int... I, class F> DEV void sfor_impl(std::integer_sequence<int, I...>, F&& f) { (f(std::integral_constant<int, I>{}), ...); }
template <int N, class F> DEV void sfor(F&& f) { sfor_impl(std::make_integer_sequence<int, N>{}, f); }

constexpr int D = 1152, KS = 36;
constexpr unsigned O_MOD = 0;
constexpr unsigned O_H = 16384;
constexpr unsigned O_WT = O_H + 64 * 36 * 2 * 1024;
constexpr unsigned O_T = O_WT + (16 * 36 * 9 + 8) * 1024;
constexpr unsigned O_VT = O_T + 32 * 36 * 4 * 1024;
constexpr unsigned O_P = O_VT + 16 * 32 * 9 * 1024;
constexpr unsigned O_ST = O_P + 128 * 32 * 1024;
constexpr unsigned O_KC = O_ST + 2048 * 16 * 4;
constexpr unsigned O_VC = O_KC + 2 * 36 * 5 * 1024;
constexpr unsigned O_WKT = O_VC + 2 * 36 * 5 * 1024;
constexpr unsigned O_VPT = O_WKT + 2 * 36 * 5 * 1024;
constexpr unsigned O_X2 = O_VPT + 2 * 72 * 3 * 1024;
constexpr unsigned O_SCP = O_X2 + 128 * 36 * 1024;
constexpr unsigned O_LNP = O_SCP + 128 * 8 * 5 * 1024;
constexpr unsigned O_U = O_LNP + 128 * 8 * 16 * 2 * 4;
constexpr unsigned WS_BYTES = O_U + 2 * 18 * 80 * 4;

DEV f32x4 mfma(v4u a, v4u b, f32x4 c) {
  return __builtin_amdgcn_mfma_f32_16x16x32_bf16(__builtin_bit_cast(bf16x8, a), __builtin_bit_cast(bf16x8, b), c, 0, 0, 0);
}
DEV unsigned pk2(float a, float b) { f32x2 v = {a, b}; return __builtin_bit_cast(unsigned, __builtin_convertvector(v, bf16x2_t)); }
DEV float asf(unsigned u) { return __builtin_bit_cast(float, u); }
DEV unsigned asu(float f) { return __builtin_bit_cast(unsigned, f); }
DEV float bflo(unsigned u) { return __builtin_bit_cast(float, u << 16); }
DEV float bfhi(unsigned u) { return __builtin_bit_cast(float, u & 0xffff0000u); }
DEV v4u pack8(f32x4 a, f32x4 b) { return v4u{pk2(a[0], a[1]), pk2(a[2], a[3]), pk2(b[0], b[1]), pk2(b[2], b[3])}; }
DEV v2u pack4(f32x4 a) { return v2u{pk2(a[0], a[1]), pk2(a[2], a[3])}; }
DEV rsrc_t mkrs(const void* p, unsigned bytes) { return __builtin_amdgcn_make_buffer_rsrc((void*)p, 0, bytes, 0x00020000); }
template <int OFF> DEV void dma_m0(unsigned m, int vo, rsrc_t rs, int so) {
  asm volatile("s_mov_b32 m0, %0\n\ts_nop 0\n\tbuffer_load_dwordx4 %1, %2, %3 offen offset:%4 lds" ::"s"(m), "v"(vo), "s"(rs), "s"(so), "i"(OFF) : "memory");
}
template <int OFF> DEV void dma(int vo, rsrc_t rs, int so) {
  asm volatile("buffer_load_dwordx4 %0, %1, %2 offen offset:%3 lds" ::"v"(vo), "s"(rs), "s"(so), "i"(OFF) : "memory");
}
template <int OFF> DEV void bld16(v4u& d, int vo, rsrc_t rs, int so) {
  asm volatile("buffer_load_dwordx4 %0, %1, %2, %3 offen offset:%4" : "=v"(d) : "v"(vo), "s"(rs), "s"(so), "i"(OFF));
}
template <int OFF> DEV void dsr16(v4u& d, unsigned addr) { asm volatile("ds_read_b128 %0, %1 offset:%2" : "=v"(d) : "v"(addr), "i"(OFF)); }
template <int OFF> DEV void dsrtr(v2u& d, unsigned addr) { asm volatile("ds_read_b64_tr_b16 %0, %1 offset:%2" : "=v"(d) : "v"(addr), "i"(OFF)); }
template <int N> DEV void waitv() { asm volatile("s_waitcnt vmcnt(%0)" ::"i"(N) : "memory"); }
template <int N> DEV void waitlv() { asm volatile("s_waitcnt vmcnt(%0) lgkmcnt(0)" ::"i"(N) : "memory"); }
DEV void waitl() { asm volatile("s_waitcnt lgkmcnt(0)" ::: "memory"); }
DEV void bar() { asm volatile("s_barrier" ::: "memory"); }
DEV void pin(v4u& x) { asm volatile("" : "+v"(x)); }
DEV void pin(v2u& x) { asm volatile("" : "+v"(x)); }
DEV void st16(v4u v, rsrc_t rs, int vo, int so) { __builtin_amdgcn_raw_buffer_store_b128(v, rs, vo, so, 0); }
DEV void st8(v2u v, rsrc_t rs, int vo, int so) { __builtin_amdgcn_raw_buffer_store_b64(v, rs, vo, so, 0); }
DEV v4u ld16(rsrc_t rs, int vo, int so) { return __builtin_amdgcn_raw_buffer_load_b128(rs, vo, so, 0); }
DEV v2u ld8(rsrc_t rs, int vo, int so) { return __builtin_amdgcn_raw_buffer_load_b64(rs, vo, so, 0); }
DEV unsigned lds_base(void* p) { return (unsigned)(size_t)(LAS char*)p; }
DEV float xmax16_32(float v) { v = fmaxf(v, __shfl_xor(v, 16)); return fmaxf(v, __shfl_xor(v, 32)); }
DEV float xsum16_32(float v) { v += __shfl_xor(v, 16); return v + __shfl_xor(v, 32); }

template <int NB, int NA, int NKS, int DIST, int BARP> struct Sched {
  static constexpr int NOCT = (NB * NKS + 7) / 8, NOPS = 2 * NOCT + NA * NKS;
  int kind[NOPS] = {}, idx[NOPS] = {}, need[NOPS] = {}, first[NKS + 2] = {}, cnt[NKS + 1] = {};
  bool isbar[NKS] = {};
  static constexpr bool bar_at(int k) { return k < 4 || k % BARP == 0; }
  constexpr Sched() {
    int n = 0;
    for (int t = -1; t < NKS; ++t) {
      for (int o = 0; o < NOCT; ++o) {
        int kb = (8 * o) / NB - 1;
        while (kb >= 0 && !bar_at(kb)) --kb;
        if (kb == t) {
          for (int h = 0; h < 2; ++h) { kind[n] = h; idx[n] = o; need[n] = t; ++n; }
          if (t >= 0) isbar[t] = true;
        }
      }
      if (t >= 0) for (int i = 0; i < NA; ++i) { kind[n] = 2; idx[n] = t * NA + i; need[n] = t; ++n; }
    }
    int p = 0;
    for (int k = -1; k < NKS; ++k) { first[k + 1] = p; while (p < NOPS && need[p] - DIST <= k && (k == -1 ? need[p] - DIST < 0 : true)) ++p; }
    first[NKS + 1] = NOPS;
    for (int k = -1; k < NKS; ++k) {
      int issued = first[k + 2 > NKS + 1 ? NKS + 1 : (k == -1 ? 1 : k + 1)], done = 0;
      while (done < NOPS && need[done] <= k) ++done;
      cnt[k + 1] = issued - done < 0 ? 0 : issued - done;
    }
  }
};

template <int NB, int NA, int NKS, int DIST, int BARP, int AW, class Mma, class Head>
DEV void ft_loop(rsrc_t rs, int sbase, int abase, unsigned lb, int w, int lane, v4u (&a)[AW][NA], Mma&& mma, Head&& head) {
  using S = Sched<NB, NA, NKS, DIST, BARP>;
  constexpr S sc{};
  static_assert(AW > DIST, "register ring too short");
  const int vo = lane * 16;
  const int sw = sbase + w * 2048;
  const unsigned mw = lb + w * 2048;
  unsigned rd[3];
  rd[0] = lb + vo; rd[1] = rd[0] + 49152; rd[2] = rd[0] + 98304;
  v4u b[2][NB];
  auto op = [&](auto oc) {
    constexpr int o = CV(oc), kind = sc.kind[o], idx = sc.idx[o];
    if constexpr (kind == 0) dma_m0<0>(mw + ((8 * idx) % 144) * 1024, vo, rs, sw + idx * 8192);
    else if constexpr (kind == 1) dma<1024>(vo, rs, sw + idx * 8192);
    else bld16<(idx & 3) * 1024>(a[(idx / NA) % AW][idx % NA], vo, rs, abase + (idx >> 2) * 4096);
  };
  auto rdb = [&](auto kc, auto jc) {
    constexpr int k = CV(kc), j = CV(jc), p = (k * NB + j) % 144;
    dsr16<(p % 48) * 1024>(b[k & 1][j], rd[p / 48]);
  };
  sfor<sc.first[1]>([&](auto oc) { op(oc); });
  waitv<sc.cnt[0]>(); bar();
  sfor<NB>([&](auto jc) { rdb(IC(0), jc); });
  SB();
  sfor<NKS>([&](auto kc) {
    constexpr int k = CV(kc), NM = NA * NB, o0 = sc.first[k + 1], no = sc.first[k + 2] - o0;
    waitlv<sc.cnt[k + 1]>();
    if constexpr (sc.isbar[k]) bar();
    sfor<NB>([&](auto jc) { pin(b[k & 1][CV(jc)]); });
    sfor<NA>([&](auto ic) { pin(a[k % AW][CV(ic)]); });
    head(kc);
    SB();
    sfor<NM>([&](auto mc) {
      constexpr int m = CV(mc), j = m / NA, i = m % NA;
      mma(kc, IC(i), IC(j), a[k % AW][i], b[k & 1][j]);
      SB();
      if constexpr (k + 1 < NKS) {
        constexpr int span = NM - NM / 4;
        sfor<NB>([&](auto rc) { constexpr int r = CV(rc); if constexpr ((r * span) / NB == m) { rdb(IC(k + 1), rc); SB(); } });
      }
      sfor<no>([&](auto qc) { constexpr int q = CV(qc); if constexpr (((2 * q + 1) * NM) / (2 * no) == m) { op(IC(o0 + q)); SB(); } });
    });
  });
}

DEV void k1_tile(int bid, int& mi, int& ni) { const int x = bid & 7, l = bid >> 3; mi = (x & 1) * 8 + (l & 7); ni = (x >> 1) * 4 + (l >> 3); }

__global__ __launch_bounds__(256) void k1(char* ws) {
  extern __shared__ __attribute__((aligned(16))) char smem[];
  const int lane = threadIdx.x & 63, w = __builtin_amdgcn_readfirstlane(threadIdx.x >> 6);
  int mi, ni; k1_tile(blockIdx.x, mi, ni);
  const rsrc_t rs = mkrs(ws, WS_BYTES);
  const int sbase = O_WT + ni * (36 * 9 * 1024), abase = O_H + (mi * 4 + w) * (72 * 1024);
  f32x4 acc[2][9];
  v4u a[4][2];
  const int vo = lane * 16;
  if (ni < 8) {
    ft_loop<9, 2, 36, 3, 2, 4>(rs, sbase, abase, lds_base(smem), w, lane, a,
      [&](auto kc, auto ic, auto jc, v4u av, v4u bv) { constexpr int i = CV(ic), j = CV(jc);
        acc[i][j] = mfma(bv, av, CV(kc) == 0 ? f32x4{0, 0, 0, 0} : acc[i][j]); }, [&](auto) {});
    const int tb = O_T + (mi * 2 + (w >> 1)) * (144 * 1024) + (w & 1) * 2048;
    const int ks0 = (9 * ni) >> 1;
    if (ni & 1) {
      sfor<2>([&](auto ic) { constexpr int i = CV(ic);
        st8(pack4(acc[i][0]), rs, vo + 8, tb + ks0 * 4096 + i * 1024);
        sfor<4>([&](auto uc) { constexpr int u = CV(uc); st16(pack8(acc[i][2 * u + 1], acc[i][2 * u + 2]), rs, vo, tb + (ks0 + 1 + u) * 4096 + i * 1024); }); });
    } else {
      sfor<2>([&](auto ic) { constexpr int i = CV(ic);
        sfor<4>([&](auto uc) { constexpr int u = CV(uc); st16(pack8(acc[i][2 * u], acc[i][2 * u + 1]), rs, vo, tb + (ks0 + u) * 4096 + i * 1024); });
        st8(pack4(acc[i][8]), rs, vo, tb + (ks0 + 4) * 4096 + i * 1024); });
    }
  } else {
    ft_loop<9, 2, 36, 3, 2, 4>(rs, sbase, abase, lds_base(smem), w, lane, a,
      [&](auto kc, auto ic, auto jc, v4u av, v4u bv) { constexpr int i = CV(ic), j = CV(jc);
        acc[i][j] = mfma(av, bv, CV(kc) == 0 ? f32x4{0, 0, 0, 0} : acc[i][j]); }, [&](auto) {});
    const int vb = O_VT + (((mi >> 3) * 8 + (ni - 8)) * 32 + (mi & 7) * 4 + w) * 9216;
    sfor<9>([&](auto jc) { constexpr int j = CV(jc); st16(pack8(acc[0][j], acc[1][j]), rs, vo, vb + j * 1024); });
  }
}

__global__ __launch_bounds__(256) void k2(char* ws) {
  extern __shared__ __attribute__((aligned(16))) char smem[];
  const int lane = threadIdx.x & 63, w = __builtin_amdgcn_readfirstlane(threadIdx.x >> 6);
  const int x = blockIdx.x & 7, l = blockIdx.x >> 3;
  const int b = x >> 2, kb = (x & 1) * 4 + (l & 3), qb = b * 16 + ((x >> 1) & 1) * 8 + (l >> 2);
  const rsrc_t rs = mkrs(ws, WS_BYTES);
  f32x4 acc[2][4];
  v4u a[6][2];
  ft_loop<4, 2, 36, 4, 2, 6>(rs, O_T + qb * (144 * 1024), O_H + ((b * 8 + kb) * 4 + w) * (72 * 1024), lds_base(smem), w, lane, a,
    [&](auto kc, auto ic, auto jc, v4u av, v4u bv) { constexpr int i = CV(ic), j = CV(jc);
      acc[i][j] = mfma(av, bv, CV(kc) == 0 ? f32x4{0, 0, 0, 0} : acc[i][j]); }, [&](auto) {});
  LAS float* red = (LAS float*)(smem + 147456);
  const float c = 0.029462782549439476f * 1.4426950408889634f;
  float mx[4];
  sfor<4>([&](auto jc) { constexpr int j = CV(jc);
    float m = fmaxf(fmaxf(fmaxf(acc[0][j][0], acc[0][j][1]), fmaxf(acc[0][j][2], acc[0][j][3])), fmaxf(fmaxf(acc[1][j][0], acc[1][j][1]), fmaxf(acc[1][j][2], acc[1][j][3])));
    mx[j] = xmax16_32(m); });
  { const int q = lane >> 4; red[w * 64 + lane] = q == 0 ? mx[0] : q == 1 ? mx[1] : q == 2 ? mx[2] : mx[3]; }
  __syncthreads();
  float sm[4];
  const int vo = lane * 16;
  sfor<4>([&](auto jc) { constexpr int j = CV(jc);
    const int o = j * 16 + (lane & 15);
    const float m = fmaxf(fmaxf(red[o], red[64 + o]), fmaxf(red[128 + o], red[192 + o])) * c;
    mx[j] = m;
    f32x4 p0, p1;
    for (int r = 0; r < 4; ++r) { p0[r] = __builtin_amdgcn_exp2f(acc[0][j][r] * c - m); p1[r] = __builtin_amdgcn_exp2f(acc[1][j][r] * c - m); }
    sm[j] = xsum16_32(((p0[0] + p0[1]) + (p0[2] + p0[3])) + ((p1[0] + p1[1]) + (p1[2] + p1[3])));
    st16(pack8(p0, p1), rs, vo, O_P + ((qb * 4 + j) * 32 + kb * 4 + w) * 1024); });
  { const int q = lane >> 4; red[256 + w * 64 + lane] = q == 0 ? sm[0] : q == 1 ? sm[1] : q == 2 ? sm[2] : sm[3]; }
  __syncthreads();
  if (w == 0) {
    const int q = lane >> 4;
    const float m = q == 0 ? mx[0] : q == 1 ? mx[1] : q == 2 ? mx[2] : mx[3];
    const float s = (red[256 + lane] + red[320 + lane]) + (red[384 + lane] + red[448 + lane]);
    st8(v2u{asu(m), asu(s)}, rs, lane * 64, O_ST + qb * 4096 + kb * 8);
  }
}

template <int PAR> DEV void k3_epi(f32x4 (&o)[9], rsrc_t rs, rsrc_t rx, int b, int nb, int qt, int lane) {
  const int r = lane & 15, q = lane >> 4, vo = lane * 16;
  const int ks0 = (9 * nb) >> 1;
  const int xo = (qt * 16 + r) * (D * 2) + ks0 * 64 + q * 16;
  const int go = O_MOD + (b * 3456 + 2304) * 2 + ks0 * 64 + q * 16;
  v4u xv[5], gv[5], wk[5][5];
  sfor<5>([&](auto uc) { constexpr int u = CV(uc); xv[u] = ld16(rx, xo, u * 64); gv[u] = ld16(rs, go, u * 64); });
  sfor<5>([&](auto uc) { constexpr int u = CV(uc); sfor<5>([&](auto tc) { constexpr int t = CV(tc);
    wk[u][t] = ld16(rs, vo, O_WKT + ((b * 36 + ks0 + u) * 5 + t) * 1024); }); });
  v4u x2[5];
  float s1 = 0.f;
  sfor<5>([&](auto uc) { constexpr int u = CV(uc);
    constexpr int tl = PAR ? 2 * u - 1 : 2 * u, th = tl + 1;
    x2[u] = v4u{0, 0, 0, 0};
    if constexpr (tl >= 0) { x2[u][0] = pk2(bflo(xv[u][0]) + bflo(gv[u][0]) * o[tl][0], bfhi(xv[u][0]) + bfhi(gv[u][0]) * o[tl][1]);
                             x2[u][1] = pk2(bflo(xv[u][1]) + bflo(gv[u][1]) * o[tl][2], bfhi(xv[u][1]) + bfhi(gv[u][1]) * o[tl][3]); }
    if constexpr (th <= 8) { x2[u][2] = pk2(bflo(xv[u][2]) + bflo(gv[u][2]) * o[th][0], bfhi(xv[u][2]) + bfhi(gv[u][2]) * o[th][1]);
                             x2[u][3] = pk2(bflo(xv[u][3]) + bflo(gv[u][3]) * o[th][2], bfhi(xv[u][3]) + bfhi(gv[u][3]) * o[th][3]); }
    for (int e = 0; e < 4; ++e) s1 += bflo(x2[u][e]) + bfhi(x2[u][e]);
    const int so = O_X2 + (qt * 36 + ks0 + u) * 1024;
    if constexpr (tl >= 0 && th <= 8) st16(x2[u], rs, vo, so);
    else if constexpr (tl >= 0) st8(v2u{x2[u][0], x2[u][1]}, rs, vo, so);
    else st8(v2u{x2[u][2], x2[u][3]}, rs, vo + 8, so); });
  const float mean = xsum16_32(s1) * (1.f / 144.f);
  float s2 = 0.f;
  sfor<5>([&](auto uc) { constexpr int u = CV(uc); constexpr int tl = PAR ? 2 * u - 1 : 2 * u, th = tl + 1;
    for (int e = (tl >= 0 ? 0 : 2); e < (th <= 8 ? 4 : 2); ++e) { const float d0 = bflo(x2[u][e]) - mean, d1 = bfhi(x2[u][e]) - mean; s2 += d0 * d0 + d1 * d1; } });
  s2 = xsum16_32(s2);
  if (lane < 16) st8(v2u{asu(mean), asu(s2)}, rs, lane * 8, O_LNP + (qt * 8 + nb) * 128);
  sfor<5>([&](auto tc) { constexpr int t = CV(tc);
    f32x4 s = {0, 0, 0, 0};
    sfor<5>([&](auto uc) { s = mfma(wk[CV(uc)][t], x2[CV(uc)], s); });
    st16(__builtin_bit_cast(v4u, s), rs, vo, O_SCP + ((qt * 8 + nb) * 5 + t) * 1024); });
}

__global__ __launch_bounds__(256) void k3(char* ws, const us* xin) {
  extern __shared__ __attribute__((aligned(16))) char smem[];
  const int lane = threadIdx.x & 63, w = __builtin_amdgcn_readfirstlane(threadIdx.x >> 6);
  const int x = blockIdx.x & 7, l = blockIdx.x >> 3;
  const int b = x >> 2, nb = (x & 1) * 4 + (l & 3), qb = b * 16 + ((x >> 1) & 1) * 8 + (l >> 2), qt = qb * 4 + w;
  const rsrc_t rs = mkrs(ws, WS_BYTES);
  v4u st[4];
  sfor<4>([&](auto ic) { bld16<CV(ic) * 16>(st[CV(ic)], (lane & 15) * 64, rs, O_ST + qt * 1024); });
  f32x4 acc[9], tot[9];
  float f[8];
  v4u a[6][1];
  ft_loop<9, 1, 32, 4, 2, 6>(rs, O_VT + (b * 8 + nb) * (288 * 1024), O_P + qt * (32 * 1024), lds_base(smem), w, lane, a,
    [&](auto kc, auto, auto jc, v4u av, v4u bv) { constexpr int j = CV(jc), k = CV(kc);
      if constexpr (k % 4 == 0 && k > 0) {
        if constexpr (k == 4) tot[j] = acc[j] * f[0]; else tot[j] += acc[j] * f[k / 4 - 1];
      }
      acc[j] = mfma(bv, av, k % 4 == 0 ? f32x4{0, 0, 0, 0} : acc[j]); },
    [&](auto kc) {
      if constexpr (CV(kc) == 1) {
        sfor<4>([&](auto ic) { pin(st[CV(ic)]); });
        float m = -3.0e38f, s = 0.f;
        for (int i = 0; i < 8; ++i) m = fmaxf(m, asf(st[i >> 1][(i & 1) * 2]));
        for (int i = 0; i < 8; ++i) { f[i] = __builtin_amdgcn_exp2f(asf(st[i >> 1][(i & 1) * 2]) - m); s += f[i] * asf(st[i >> 1][(i & 1) * 2 + 1]); }
        s = 1.f / s;
        for (int i = 0; i < 8; ++i) f[i] *= s;
      } });
  sfor<9>([&](auto jc) { tot[CV(jc)] += acc[CV(jc)] * f[7]; });
  const rsrc_t rx = mkrs(xin, 2048 * D * 2);
  if (nb & 1) k3_epi<1>(tot, rs, rx, b, nb, qt, lane); else k3_epi<0>(tot, rs, rx, b, nb, qt, lane);
}

template <int PAR> DEV void k4_out(v4u (&vp)[9][3], v4u (&pc)[3], v4u (&x2)[5], rsrc_t ro, int nb, int qt, int lane) {
  const int r = lane & 15, q = lane >> 4, ks0 = (9 * nb) >> 1;
  f32x4 o[9];
  sfor<9>([&](auto tc) { constexpr int t = CV(tc); o[t] = mfma(vp[t][2], pc[2], mfma(vp[t][1], pc[1], mfma(vp[t][0], pc[0], f32x4{0, 0, 0, 0}))); });
  const int oo = (qt * 16 + r) * (D * 2) + ks0 * 64 + q * 16;
  sfor<5>([&](auto uc) { constexpr int u = CV(uc); constexpr int tl = PAR ? 2 * u - 1 : 2 * u, th = tl + 1;
    v4u y = {0, 0, 0, 0};
    if constexpr (tl >= 0) { y[0] = pk2(bflo(x2[u][0]) + o[tl][0], bfhi(x2[u][0]) + o[tl][1]); y[1] = pk2(bflo(x2[u][1]) + o[tl][2], bfhi(x2[u][1]) + o[tl][3]); }
    if constexpr (th <= 8) { y[2] = pk2(bflo(x2[u][2]) + o[th][0], bfhi(x2[u][2]) + o[th][1]); y[3] = pk2(bflo(x2[u][3]) + o[th][2], bfhi(x2[u][3]) + o[th][3]); }
    if constexpr (tl >= 0 && th <= 8) st16(y, ro, oo, u * 64);
    else if constexpr (tl >= 0) st8(v2u{y[0], y[1]}, ro, oo, u * 64);
    else st8(v2u{y[2], y[3]}, ro, oo + 8, u * 64); });
}

__global__ __launch_bounds__(256) void k4(char* ws, us* out) {
  __shared__ f32x4 red[4][5][64];
  __shared__ float ured[4][80];
  const int lane = threadIdx.x & 63, w = __builtin_amdgcn_readfirstlane(threadIdx.x >> 6);
  const int x = blockIdx.x & 7, l = blockIdx.x >> 3;
  const int b = x >> 2, qt = b * 64 + ((x >> 1) & 1) * 32 + l, nb = (x & 1) * 4 + w;
  const rsrc_t rs = mkrs(ws, WS_BYTES), ro = mkrs(out, 2048 * D * 2);
  const int vo = lane * 16, r = lane & 15, q = lane >> 4;
  v4u sp[2][5], vp[9][3], x2[5];
  sfor<2>([&](auto hc) { sfor<5>([&](auto tc) { sp[CV(hc)][CV(tc)] = ld16(rs, vo, O_SCP + ((qt * 8 + 2 * w + CV(hc)) * 5 + CV(tc)) * 1024); }); });
  v2u lp[8];
  sfor<8>([&](auto ic) { lp[CV(ic)] = ld8(rs, r * 8, O_LNP + (qt * 8 + CV(ic)) * 128); });
  if (threadIdx.x < 240) { const int g = threadIdx.x / 80, m = threadIdx.x % 80; const float* up = (const float*)(ws + O_U) + (b * 18 + g * 6) * 80 + m;
    ured[g][m] = ((up[0] + up[80]) + (up[160] + up[240])) + (up[320] + up[400]); }
  sfor<5>([&](auto tc) { constexpr int t = CV(tc); red[w][t][lane] = __builtin_bit_cast(f32x4, sp[0][t]) + __builtin_bit_cast(f32x4, sp[1][t]); });
  sfor<9>([&](auto tc) { sfor<3>([&](auto kc) { vp[CV(tc)][CV(kc)] = ld16(rs, vo, O_VPT + ((b * 72 + 9 * nb + CV(tc)) * 3 + CV(kc)) * 1024); }); });
  sfor<5>([&](auto uc) { x2[CV(uc)] = ld16(rs, vo, O_X2 + (qt * 36 + ((9 * nb) >> 1) + CV(uc)) * 1024); });
  float mean = 0.f;
  for (int i = 0; i < 8; ++i) mean += asf(lp[i][0]);
  mean *= 0.125f;
  float m2 = 0.f;
  for (int i = 0; i < 8; ++i) { const float d = asf(lp[i][0]) - mean; m2 += asf(lp[i][1]) + 144.f * d * d; }
  const float c = 0.029462782549439476f * 1.4426950408889634f / sqrtf(m2 * (1.f / D) + 1e-5f);
  __syncthreads();
  f32x4 s[5];
  float mx = -3.0e38f;
  sfor<5>([&](auto tc) { constexpr int t = CV(tc);
    s[t] = (red[0][t][lane] + red[1][t][lane]) + (red[2][t][lane] + red[3][t][lane]);
    for (int e = 0; e < 4; ++e) {
      const int m = t * 16 + q * 4 + e;
      const float u = (ured[0][m] + ured[1][m]) + ured[2][m];
      s[t][e] = m < 77 ? (s[t][e] - mean * u) * c : -3.0e38f;
      mx = fmaxf(mx, s[t][e]);
    } });
  mx = xmax16_32(mx);
  float sum = 0.f;
  sfor<5>([&](auto tc) { constexpr int t = CV(tc); for (int e = 0; e < 4; ++e) { s[t][e] = __builtin_amdgcn_exp2f(s[t][e] - mx); sum += s[t][e]; } });
  sum = 1.f / xsum16_32(sum);
  v4u pc[3];
  pc[0] = pack8(s[0] * sum, s[1] * sum); pc[1] = pack8(s[2] * sum, s[3] * sum); pc[2] = pack8(s[4] * sum, f32x4{0, 0, 0, 0});
  if (nb & 1) k4_out<1>(vp, pc, x2, ro, nb, qt, lane); else k4_out<0>(vp, pc, x2, ro, nb, qt, lane);
}

template <int NK, int NP, int NR, int NM, class Issue, class Rd, class Pin, class Mm>
DEV void raw_loop(Issue&& issue, Rd&& rd, Pin&& pinf, Mm&& mm) {
  constexpr int NS = KS / NK;
  sfor<3>([&](auto sc) { sfor<NP>([&](auto pc) { issue(sc, pc); }); });
  waitv<2 * NP>(); bar();
  sfor<NR>([&](auto rc) { rd(IC(0), rc); });
  SB();
  sfor<KS>([&](auto kc) {
    constexpr int k = CV(kc), s = k / NK, kk = k % NK;
    constexpr int p0 = (kk * NP) / NK, p1 = ((kk + 1) * NP) / NK, np = (s + 3 < NS) ? p1 - p0 : 0;
    if constexpr (kk == NK - 1 && s + 1 < NS) {
      constexpr int later = (s + 2 < NS ? NP : 0) + (s + 3 < NS ? p0 : 0);
      waitlv<later>(); bar();
    } else waitl();
    pinf(kc);
    SB();
    sfor<NM>([&](auto mc) {
      constexpr int m = CV(mc);
      mm(kc, mc);
      SB();
      if constexpr (k + 1 < KS) {
        constexpr int span = NM - NM / 4;
        sfor<NR>([&](auto rc) { if constexpr ((CV(rc) * span) / NR == m) { rd(IC(k + 1), rc); SB(); } });
      }
      sfor<np>([&](auto qc) { if constexpr (((2 * CV(qc) + 1) * NM) / (2 * np) == m) { issue(IC(s + 3), IC(p0 + CV(qc))); SB(); } });
    });
  });
}

DEV unsigned kc_rd(unsigned base, int lane, int h) { const int r = lane & 15, q = lane >> 4; return base + r * 128 + (((4 * h + q) ^ (r & 7)) << 4); }
template <bool PI> DEV int kc_src(int lane, int stride) { const int rr = lane >> 3; return (PI ? 8 * (rr >> 2) + (rr & 3) : rr) * stride + (((lane & 7) ^ rr) << 4); }
constexpr int pi_row(int p) { return 32 * (p >> 2) + 16 * (p & 1) + 4 * ((p >> 1) & 1); }
DEV int pi_inv_off(int t, int lane, int blk0, int blk1) {
  const int xx = 16 * (t & 1) + (lane & 15);
  return (((xx >> 2) & 1) ? blk1 : blk0) + (((lane >> 4) * 16 + 4 * (xx >> 3) + (xx & 3)) << 4);
}

template <int TRB> DEV void w_tile(char* ws, const us* wqkv, const us* wo, int ti, int tj, char* smem) {
  const int lane = threadIdx.x & 63, w = __builtin_amdgcn_readfirstlane(threadIdx.x >> 6), wr = w >> 1, wc = w & 1;
  const rsrc_t ra = mkrs(wqkv, D * 3456 * 2), rb = TRB ? mkrs(wo, D * D * 2) : ra;
  const unsigned lb = lds_base(smem);
  const int va = kc_src<true>(lane, 6912);
  const int sa = ti * 128 * 6912 + (TRB ? 4608 : 0) + (16 * (w & 1) + 4 * (w >> 1)) * 6912;
  int vb, sb;
  if constexpr (TRB) { vb = (lane >> 4) * 2304 + (((lane & 15) ^ (((lane >> 4) << 2) | w)) << 4); sb = tj * 256 + 4 * w * 2304; }
  else { vb = kc_src<false>(lane, 6912); sb = (tj * 128 + 8 * w) * 6912 + 2304; }
  unsigned ra_[2][2], rb_[2][8];
  sfor<2>([&](auto gc) { constexpr int g = CV(gc);
    sfor<2>([&](auto hc) { ra_[g][CV(hc)] = kc_rd(lb + g * 65536 + wr * 8192, lane, CV(hc)); });
    if constexpr (TRB) {
      const int gg = lane >> 4, qq = (lane & 15) >> 2, pp = lane & 3;
      sfor<8>([&](auto ec) { constexpr int c = CV(ec) >> 1, t = CV(ec) & 1;
        const int row = 8 * gg + 4 * t + qq, ch = 2 * (4 * wc + c) + (pp >> 1);
        rb_[g][CV(ec)] = lb + g * 65536 + 16384 + 256 * row + 16 * (ch ^ ((qq << 2) | ((2 * gg + t) & 3))) + 8 * (pp & 1); });
    } else sfor<2>([&](auto hc) { rb_[g][CV(hc)] = kc_rd(lb + g * 65536 + 16384 + wc * 8192, lane, CV(hc)); }); });
  v4u fa[2][4], fb[2][4];
  v2u ft[2][8];
  f32x4 acc[4][4];
  raw_loop<2, 8, TRB ? 12 : 8, 16>(
    [&](auto sc, auto pc) { constexpr int s = CV(sc), p = CV(pc), u = p >> 1;
      const unsigned m = lb + (s & 3) * 32768 + (p & 1) * 16384 + u * 4096 + w * 1024;
      if constexpr ((p & 1) == 0) dma_m0<0>(m, va, ra, sa + u * 32 * 6912 + s * 128);
      else if constexpr (TRB) dma_m0<0>(m, vb, rb, sb + (u * 16 + s * 64) * 2304);
      else dma_m0<0>(m, vb, rb, sb + u * 32 * 6912 + s * 128); },
    [&](auto kc, auto rc) { constexpr int k = CV(kc), r = CV(rc), s = (k >> 1) & 3, h = k & 1;
      if constexpr (r < 4) dsr16<(s & 1) * 32768 + r * 2048>(fa[k & 1][r], ra_[s >> 1][h]);
      else if constexpr (TRB) dsrtr<(s & 1) * 32768 + h * 8192>(ft[k & 1][r - 4], rb_[s >> 1][r - 4]);
      else dsr16<(s & 1) * 32768 + (r - 4) * 2048>(fb[k & 1][r - 4], rb_[s >> 1][h]); },
    [&](auto kc) { constexpr int k = CV(kc);
      sfor<4>([&](auto ic) { pin(fa[k & 1][CV(ic)]); });
      if constexpr (TRB) sfor<4>([&](auto ic) { constexpr int i = CV(ic); pin(ft[k & 1][2 * i]); pin(ft[k & 1][2 * i + 1]);
        fb[k & 1][i] = v4u{ft[k & 1][2 * i][0], ft[k & 1][2 * i][1], ft[k & 1][2 * i + 1][0], ft[k & 1][2 * i + 1][1]}; });
      else sfor<4>([&](auto ic) { pin(fb[k & 1][CV(ic)]); }); },
    [&](auto kc, auto mc) { constexpr int k = CV(kc), i = CV(mc) & 3, j = CV(mc) >> 2;
      acc[i][j] = mfma(fa[k & 1][i], fb[k & 1][j], k == 0 ? f32x4{0, 0, 0, 0} : acc[i][j]); });
  const rsrc_t rs = mkrs(ws, WS_BYTES);
  sfor<4>([&](auto jc) { constexpr int j = CV(jc);
    const int T0 = TRB * 72 + tj * 8 + wc * 4 + (j & 2), T1 = T0 + 1;
    const int b0 = (T0 / 9) * (324 * 1024) + (T0 % 9) * 1024, b1 = (T1 / 9) * (324 * 1024) + (T1 % 9) * 1024;
    const int off = pi_inv_off(j, lane, b0, b1);
    sfor<2>([&](auto vc) { constexpr int v = CV(vc); st16(pack8(acc[2 * v][j], acc[2 * v + 1][j]), rs, off, O_WT + (ti * 4 + wr * 2 + v) * 9216); }); });
}

template <int TR, int FT, bool W64_IS_A>
DEV void small_tile(f32x4 (&acc)[5], rsrc_t r64, int s64, int stride64, rsrc_t r80, int s80, char* smem) {
  const int lane = threadIdx.x & 63, w = __builtin_amdgcn_readfirstlane(threadIdx.x >> 6);
  const unsigned lb = lds_base(smem);
  int v64, sw64, v80, v80x = 0, sw80;
  if constexpr (TR) { const int rr = lane >> 3; v64 = rr * stride64 + (((lane & 7) ^ ((((rr >> 1) & 1) | ((w & 1) << 1)) << 1)) << 4); sw64 = s64 + 8 * w * stride64; }
  else { v64 = kc_src<true>(lane, stride64); sw64 = s64 + (16 * (w & 1) + 4 * (w >> 1)) * stride64; }
  if constexpr (FT) { v80 = lane * 16; sw80 = s80 + w * 5120; }
  else {
    v80 = kc_src<false>(lane, D * 2);
    v80x = ((lane >> 3) < 5 || !(w & 1)) ? v80 : 0x7ff00000;
    sw80 = s80;
  }
  unsigned r64a[3][2], r80a[3][2];
  sfor<3>([&](auto gc) { constexpr int g = CV(gc); const unsigned base = lb + g * 49152;
    if constexpr (TR) { const int gg = lane >> 4, qq = (lane & 15) >> 2, pp = lane & 3;
      r64a[g][0] = base + (8 * gg + qq) * 128 + (((2 * w + (pp >> 1)) ^ (((qq >> 1) | ((gg & 1) << 1)) << 1)) << 4) + 8 * (pp & 1); }
    else sfor<2>([&](auto hc) { r64a[g][CV(hc)] = kc_rd(base + w * 2048, lane, CV(hc)); });
    if constexpr (FT) r80a[g][0] = base + lane * 16; else sfor<2>([&](auto hc) { r80a[g][CV(hc)] = kc_rd(base, lane, CV(hc)); }); });
  v4u f64[2], f80[2][5];
  v2u ft[2][2];
  raw_loop<4, 9, TR ? 7 : 6, 5>(
    [&](auto sc, auto pc) { constexpr int s = CV(sc), p = CV(pc);
      const unsigned m = lb + (s & 3) * 36864;
      if constexpr (p < 4) {
        if constexpr (TR) dma_m0<0>(m + (w + 4 * p) * 1024, v64, r64, sw64 + (s * 128 + 32 * p) * stride64);
        else dma_m0<0>(m + (p >> 1) * 8192 + (w + 4 * (p & 1)) * 1024, v64, r64, sw64 + 32 * (p & 1) * stride64 + s * 256 + (p >> 1) * 128);
      } else {
        constexpr int e = p - 4;
        if constexpr (FT) dma_m0<0>(m + 16384 + (5 * w + e) * 1024, v80, r80, sw80 + s * 20480 + e * 1024);
        else { const int n = 5 * w + e, hf = n >= 10, g = n - 10 * hf;
          dma_m0<0>(m + 16384 + n * 1024, e == 4 ? v80x : v80, r80, sw80 + g * 8 * (D * 2) + s * 256 + hf * 128); }
      } },
    [&](auto kc, auto rc) { constexpr int k = CV(kc), r = CV(rc), kk = k & 3, so = ((k >> 2) & 3) * 36864;
      if constexpr (r < 5) {
        constexpr int o = so + 16384 + (FT ? (kk * 5 + r) * 1024 : (kk >> 1) * 10240 + r * 2048);
        dsr16<o % 49152>(f80[k & 1][r], r80a[o / 49152][FT ? 0 : (kk & 1)]);
      } else if constexpr (TR) { constexpr int o = so + kk * 4096 + (r - 5) * 512; dsrtr<o % 49152>(ft[k & 1][r - 5], r64a[o / 49152][0]); }
      else { constexpr int o = so + (kk >> 1) * 8192; dsr16<o % 49152>(f64[k & 1], r64a[o / 49152][kk & 1]); } },
    [&](auto kc) { constexpr int k = CV(kc);
      sfor<5>([&](auto ic) { pin(f80[k & 1][CV(ic)]); });
      if constexpr (TR) { pin(ft[k & 1][0]); pin(ft[k & 1][1]); f64[k & 1] = v4u{ft[k & 1][0][0], ft[k & 1][0][1], ft[k & 1][1][0], ft[k & 1][1][1]}; }
      else pin(f64[k & 1]); },
    [&](auto kc, auto mc) { constexpr int k = CV(kc), m = CV(mc);
      const f32x4 c = k == 0 ? f32x4{0, 0, 0, 0} : acc[m];
      acc[m] = W64_IS_A ? mfma(f64[k & 1], f80[k & 1][m], c) : mfma(f80[k & 1][m], f64[k & 1], c); });
}

DEV void kvc_tile(char* ws, const us* y, const us* wkv, int b, int tn, char* smem) {
  const int lane = threadIdx.x & 63, w = __builtin_amdgcn_readfirstlane(threadIdx.x >> 6);
  f32x4 acc[5];
  small_tile<1, 0, true>(acc, mkrs(wkv, D * 2304 * 2), tn * 128, 4608, mkrs(y, 154 * D * 2), b * 77 * D * 2, smem);
  const rsrc_t rs = mkrs(ws, WS_BYTES);
  const int isv = tn >= 18, n0 = (tn - 18 * isv) * 64 + 16 * w, q = lane >> 4;
  const int vo = ((2 * (w & 1) + (q >> 1)) * 16 + (lane & 15)) * 16 + (q & 1) * 8;
  sfor<5>([&](auto tc) { st8(pack4(acc[CV(tc)]), rs, vo, (isv ? O_VC : O_KC) + ((b * 36 + (n0 >> 5)) * 5 + CV(tc)) * 1024); });
}

DEV void ln_tile(char* ws, const us* xin, int rt, char* smem) {
  const int lane = threadIdx.x & 63, w = __builtin_amdgcn_readfirstlane(threadIdx.x >> 6), r = lane & 15, q = lane >> 4;
  LAS float* red = (LAS float*)smem;
  const rsrc_t rs = mkrs(ws, WS_BYTES), rx = mkrs(xin, 2048 * D * 2);
  v4u xv[9], sh[9], sc[9];
  sfor<9>([&](auto kc) { xv[CV(kc)] = ld16(rx, (rt * 16 + r) * (D * 2) + q * 16, (9 * w + CV(kc)) * 64); });
  sfor<9>([&](auto kc) { sh[CV(kc)] = ld16(rs, q * 16, O_MOD + (rt >> 6) * 6912 + (9 * w + CV(kc)) * 64); sc[CV(kc)] = ld16(rs, q * 16, O_MOD + (rt >> 6) * 6912 + 2304 + (9 * w + CV(kc)) * 64); });
  float s = 0.f;
  sfor<9>([&](auto kc) { for (int e = 0; e < 4; ++e) s += bflo(xv[CV(kc)][e]) + bfhi(xv[CV(kc)][e]); });
  s = xsum16_32(s);
  if (lane < 16) red[w * 16 + lane] = s;
  __syncthreads();
  const float mean = ((red[r] + red[16 + r]) + (red[32 + r] + red[48 + r])) * (1.f / D);
  s = 0.f;
  sfor<9>([&](auto kc) { for (int e = 0; e < 4; ++e) { const float d0 = bflo(xv[CV(kc)][e]) - mean, d1 = bfhi(xv[CV(kc)][e]) - mean; s += d0 * d0 + d1 * d1; } });
  s = xsum16_32(s);
  if (lane < 16) red[64 + w * 16 + lane] = s;
  __syncthreads();
  const float rstd = 1.f / sqrtf(((red[64 + r] + red[80 + r]) + (red[96 + r] + red[112 + r])) * (1.f / D) + 1e-5f);
  sfor<9>([&](auto kc) { constexpr int k = CV(kc);
    v4u o;
    for (int e = 0; e < 4; ++e)
      o[e] = pk2((bflo(xv[k][e]) - mean) * rstd * (1.f + bflo(sc[k][e])) + bflo(sh[k][e]), (bfhi(xv[k][e]) - mean) * rstd * (1.f + bfhi(sc[k][e])) + bfhi(sh[k][e]));
    st16(o, rs, lane * 16, O_H + (((rt >> 1) * 36 + 9 * w + k) * 2 + (rt & 1)) * 1024); });
}

__global__ __launch_bounds__(256) void k_mod(char* ws, const us* cin, const us* wmod) {
  __shared__ float sc[2][D];
  __shared__ float red[4][2][64];
  const int tid = threadIdx.x, lane = tid & 63, w = tid >> 6;
  const rsrc_t rw = mkrs(wmod, D * 6912 * 2);
  v4u wv[36];
  sfor<36>([&](auto ic) { wv[CV(ic)] = ld16(rw, (tid >> 3) * 13824 + (tid & 7) * 16, blockIdx.x * 128 + CV(ic) * 32 * 13824); });
  for (int i = tid; i < 2 * D; i += 256) { const float v = bflo(cin[i]); sc[0][i] = bflo(pk2(v / (1.f + __expf(-v)), 0.f)); }
  __syncthreads();
  float acc[2][8] = {};
  sfor<36>([&](auto ic) { constexpr int i = CV(ic);
    const float c0 = sc[0][i * 32 + (tid >> 3)], c1 = sc[1][i * 32 + (tid >> 3)];
    for (int e = 0; e < 4; ++e) { const float lo = bflo(wv[i][e]), hi = bfhi(wv[i][e]);
      acc[0][2 * e] += c0 * lo; acc[0][2 * e + 1] += c0 * hi; acc[1][2 * e] += c1 * lo; acc[1][2 * e + 1] += c1 * hi; } });
  for (int bb = 0; bb < 2; ++bb) for (int e = 0; e < 8; ++e) {
    float v = acc[bb][e]; v += __shfl_xor(v, 8); v = xsum16_32(v);
    if (lane < 8) red[w][bb][lane * 8 + e] = v; }
  __syncthreads();
  if (tid < 128) { const int bb = tid >> 6; const float v = (red[0][bb][lane] + red[1][bb][lane]) + (red[2][bb][lane] + red[3][bb][lane]);
    ((us*)(ws + O_MOD))[bb * 3456 + blockIdx.x * 64 + lane] = (us)pk2(v, 0.f); }
}

__global__ __launch_bounds__(256) void k0(char* ws, const us* xin, const us* y, const us* wqkv, const us* wo, const us* wkv) {
  extern __shared__ __attribute__((aligned(16))) char smem[];
  const int bid = blockIdx.x;
  if (bid < 81) w_tile<0>(ws, wqkv, wo, bid % 9, bid / 9, smem);
  else if (bid < 162) w_tile<1>(ws, wqkv, wo, (bid - 81) % 9, (bid - 81) / 9, smem);
  else if (bid < 234) kvc_tile(ws, y, wkv, (bid - 162) & 1, (bid - 162) >> 1, smem);
}
__global__ __launch_bounds__(256) void k_ln(char* ws, const us* xin) {
  __shared__ __attribute__((aligned(16))) char smem[512];
  ln_tile(ws, xin, blockIdx.x, smem);
}

__global__ __launch_bounds__(256) void k_cross(char* ws, const us* wq_c, const us* wo_c) {
  extern __shared__ __attribute__((aligned(16))) char smem[];
  const int lane = threadIdx.x & 63, w = __builtin_amdgcn_readfirstlane(threadIdx.x >> 6);
  const int b = blockIdx.x & 1, t = blockIdx.x >> 1;
  const rsrc_t rs = mkrs(ws, WS_BYTES);
  f32x4 acc[5];
  if (t < 18) {
    small_tile<0, 1, true>(acc, mkrs(wq_c, D * D * 2), t * 64 * (D * 2), D * 2, rs, O_KC + b * (180 * 1024), smem);
    LAS float* red = (LAS float*)(smem + 147456);
    sfor<5>([&](auto tc) { const v2u v = pack4(acc[CV(tc)]);
      st8(v, rs, lane * 16 + (w & 1) * 8, O_WKT + ((b * 36 + 2 * t + (w >> 1)) * 5 + CV(tc)) * 1024);
      const float s = xsum16_32((bflo(v[0]) + bfhi(v[0])) + (bflo(v[1]) + bfhi(v[1])));
      if (lane < 16) red[w * 80 + CV(tc) * 16 + lane] = s; });
    __syncthreads();
    if (threadIdx.x < 80) ((float*)(ws + O_U))[(b * 18 + t) * 80 + threadIdx.x] = (red[threadIdx.x] + red[80 + threadIdx.x]) + (red[160 + threadIdx.x] + red[240 + threadIdx.x]);
  } else {
    const int td = t - 18;
    small_tile<1, 1, false>(acc, mkrs(wo_c, D * D * 2), td * 128, D * 2, rs, O_VC + b * (180 * 1024), smem);
    const int T0 = b * 72 + td * 4 + (w & 2);
    const int off = pi_inv_off(w, lane, T0 * 3072, (T0 + 1) * 3072);
    st16(pack8(acc[0], acc[1]), rs, off, O_VPT);
    st16(pack8(acc[2], acc[3]), rs, off, O_VPT + 1024);
    st16(pack8(acc[4], f32x4{0, 0, 0, 0}), rs, off, O_VPT + 2048);
  }
}
}

extern "C" void kernel_launch(void* const* d_in, const int* in_sizes, int n_in, void* d_out, int out_size, void* d_ws, size_t ws_size, hipStream_t stream) {
  const us* x = (const us*)d_in[0]; const us* c = (const us*)d_in[1]; const us* y = (const us*)d_in[2];
  const us* wqkv = (const us*)d_in[3]; const us* wo = (const us*)d_in[4]; const us* wq_c = (const us*)d_in[5];
  const us* wkv_c = (const us*)d_in[6]; const us* wo_c = (const us*)d_in[7]; const us* w_mod = (const us*)d_in[8];
  char* ws = (char*)d_ws;
  constexpr int L = 147456 + 2048;
  (void)hipFuncSetAttribute((const void*)k0, hipFuncAttributeMaxDynamicSharedMemorySize, L);
  (void)hipFuncSetAttribute((const void*)k_cross, hipFuncAttributeMaxDynamicSharedMemorySize, L);
  (void)hipFuncSetAttribute((const void*)k1, hipFuncAttributeMaxDynamicSharedMemorySize, L);
  (void)hipFuncSetAttribute((const void*)k2, hipFuncAttributeMaxDynamicSharedMemorySize, L);
  (void)hipFuncSetAttribute((const void*)k3, hipFuncAttributeMaxDynamicSharedMemorySize, L);
  k_mod<<<54, 256, 0, stream>>>(ws, c, w_mod);
  k_ln<<<128, 256, 0, stream>>>(ws, x);
  k0<<<234, 256, L, stream>>>(ws, x, y, wqkv, wo, wkv_c);
  k_cross<<<72, 256, L, stream>>>(ws, wq_c, wo_c);
  k1<<<256, 256, L, stream>>>(ws);
  k2<<<256, 256, L, stream>>>(ws);
  k3<<<256, 256, L, stream>>>(ws, x);
  k4<<<256, 256, 0, stream>>>(ws, (us*)d_out);
}
```

```cpp
#include <hip/hip_runtime.h>
#include <utility>
#include <stdint.h>

namespace {
typedef unsigned v4u __attribute__((ext_vector_type(4)));
typedef unsigned v2u __attribute__((ext_vector_type(2)));
typedef float f32x4 __attribute__((ext_vector_type(4)));
typedef float f32x2 __attribute__((ext_vector_type(2)));
typedef short bf16x8 __attribute__((ext_vector_type(8)));
typedef __bf16 bf16x2_t __attribute__((ext_vector_type(2)));
typedef __amdgpu_buffer_rsrc_t rsrc_t;
typedef unsigned short us;
#define LAS __attribute__((address_space(3)))
#define DEV __device__ __forceinline__
#define SB() __builtin_amdgcn_sched_barrier(0)
#define IC(x) std::integral_constant<int, (x)>{}
#define CV(x) decltype(x)::value

template <int B, int... I, class F> DEV void sfor_impl(std::integer_sequence<int, I...>, F&& f) { (f(std::integral_constant<int, B + I>{}), ...); }
template <int N, class F> DEV void sfor(F&& f) { sfor_impl<0>(std::make_integer_sequence<int, N>{}, f); }
template <int B, int E, class F> DEV void srange(F&& f) { sfor_impl<B>(std::make_integer_sequence<int, (E > B ? E - B : 0)>{}, f); }

constexpr int D = 1152, KS = 36;
constexpr unsigned O_EP = 0;
constexpr unsigned O_FL = 256;
constexpr int F_MOD = 0, F_H = 64, F_W = 192, F_KVC = 368, F_X = 416, F_T = 512, F_V = 768, F_P = 1024, F_S = 2048, F_END = 3072;
constexpr unsigned O_MOD = 32768;
constexpr unsigned O_H = O_MOD + 16384;
constexpr unsigned O_WT = O_H + 64 * 72 * 1024;
constexpr unsigned O_T = O_WT + (16 * 324 + 8) * 1024;
constexpr unsigned O_VT = O_T + 32 * 144 * 1024;
constexpr unsigned O_P = O_VT + 16 * 288 * 1024;
constexpr unsigned O_ST = O_P + 128 * 32 * 1024;
constexpr unsigned O_KC = O_ST + 128 * 4096;
constexpr unsigned O_VC = O_KC + 360 * 1024;
constexpr unsigned O_WKT = O_VC + 360 * 1024;
constexpr unsigned O_VPT = O_WKT + 368 * 1024;
constexpr unsigned O_X2 = O_VPT + 432 * 1024;
constexpr unsigned O_SCP = O_X2 + 128 * 36 * 1024;
constexpr unsigned O_LNP = O_SCP + 128 * 40 * 1024;
constexpr unsigned O_U = O_LNP + 128 * 1024;
constexpr unsigned WS_BYTES = O_U + 2 * 18 * 80 * 4;
constexpr int LDS_MISC = 147456, LDS_BYTES = 163840;
constexpr int SC1 = 16;

#define STAMP(k, s)

DEV f32x4 mfma(v4u a, v4u b, f32x4 c) {
  return __builtin_amdgcn_mfma_f32_16x16x32_bf16(__builtin_bit_cast(bf16x8, a), __builtin_bit_cast(bf16x8, b), c, 0, 0, 0);
}
DEV unsigned pk2(float a, float b) { f32x2 v = {a, b}; return __builtin_bit_cast(unsigned, __builtin_convertvector(v, bf16x2_t)); }
DEV float asf(unsigned u) { return __builtin_bit_cast(float, u); }
DEV unsigned asu(float f) { return __builtin_bit_cast(unsigned, f); }
DEV float bflo(unsigned u) { return __builtin_bit_cast(float, u << 16); }
DEV float bfhi(unsigned u) { return __builtin_bit_cast(float, u & 0xffff0000u); }
DEV v4u pack8(f32x4 a, f32x4 b) { return v4u{pk2(a[0], a[1]), pk2(a[2], a[3]), pk2(b[0], b[1]), pk2(b[2], b[3])}; }
DEV v2u pack4(f32x4 a) { return v2u{pk2(a[0], a[1]), pk2(a[2], a[3])}; }
DEV rsrc_t mkrs(const void* p, unsigned bytes) { return __builtin_amdgcn_make_buffer_rsrc((void*)p, 0, bytes, 0x00020000); }
DEV void dma_m0(unsigned m, int vo, rsrc_t rs, int so) {
  asm volatile("s_mov_b32 m0, %0\n\ts_nop 0\n\tbuffer_load_dwordx4 %1, %2, %3 offen lds" ::"s"(m), "v"(vo), "s"(rs), "s"(so) : "memory");
}
DEV void dma_m0_h(unsigned m, int vo, rsrc_t rs, int so) {
  asm volatile("s_mov_b32 m0, %0\n\ts_nop 0\n\tbuffer_load_dwordx4 %1, %2, %3 offen sc1 lds" ::"s"(m), "v"(vo), "s"(rs), "s"(so) : "memory");
}
DEV void dma_2nd_h(int vo, rsrc_t rs, int so) {
  asm volatile("buffer_load_dwordx4 %0, %1, %2 offen offset:1024 sc1 lds" ::"v"(vo), "s"(rs), "s"(so) : "memory");
}
template <int OFF> DEV void ald16(v4u& d, int vo, rsrc_t rs, int so) {
  asm volatile("buffer_load_dwordx4 %0, %1, %2, %3 offen offset:%4 sc1" : "=v"(d) : "v"(vo), "s"(rs), "s"(so), "i"(OFF));
}
template <int OFF> DEV void ald16a(v4u& d, int vo, rsrc_t rs, int so) {
  asm volatile("buffer_load_dwordx4 %0, %1, %2, %3 offen offset:%4 sc1" : "=a"(d) : "v"(vo), "s"(rs), "s"(so), "i"(OFF));
}
template <int OFF> DEV void ald8(v2u& d, int vo, rsrc_t rs, int so) {
  asm volatile("buffer_load_dwordx2 %0, %1, %2, %3 offen offset:%4 sc1" : "=v"(d) : "v"(vo), "s"(rs), "s"(so), "i"(OFF));
}
template <int OFF> DEV void dsr16(v4u& d, unsigned addr) { asm volatile("ds_read_b128 %0, %1 offset:%2" : "=v"(d) : "v"(addr), "i"(OFF)); }
template <int OFF> DEV void dsrtr(v2u& d, unsigned addr) { asm volatile("ds_read_b64_tr_b16 %0, %1 offset:%2" : "=v"(d) : "v"(addr), "i"(OFF)); }
template <int N> DEV void waitv() { asm volatile("s_waitcnt vmcnt(%0)" ::"i"(N) : "memory"); }
template <int N> DEV void waitlv() { asm volatile("s_waitcnt vmcnt(%0) lgkmcnt(0)" ::"i"(N) : "memory"); }
DEV void waitl() { asm volatile("s_waitcnt lgkmcnt(0)" ::: "memory"); }
DEV void bar() { asm volatile("s_barrier" ::: "memory"); }
DEV void lbar() { asm volatile("s_waitcnt lgkmcnt(0)\n\ts_barrier" ::: "memory"); }
DEV void pin(v4u& x) { asm volatile("" : "+v"(x)); }
DEV void pin(v2u& x) { asm volatile("" : "+v"(x)); }
DEV void st16(v4u v, rsrc_t rs, int vo, int so) { __builtin_amdgcn_raw_buffer_store_b128(v, rs, vo, so, SC1); }
DEV void st8(v2u v, rsrc_t rs, int vo, int so) { __builtin_amdgcn_raw_buffer_store_b64(v, rs, vo, so, SC1); }
DEV v4u ld16(rsrc_t rs, int vo, int so) { return __builtin_amdgcn_raw_buffer_load_b128(rs, vo, so, 0); }
DEV v4u ld16h(rsrc_t rs, int vo, int so) { return __builtin_amdgcn_raw_buffer_load_b128(rs, vo, so, SC1); }
DEV float xmax16_32(float v) { v = fmaxf(v, __shfl_xor(v, 16)); return fmaxf(v, __shfl_xor(v, 32)); }
DEV float xsum16_32(float v) { v += __shfl_xor(v, 16); return v + __shfl_xor(v, 32); }
template <bool HA, bool H_IS_A, bool FIRST> DEV void mma_h(f32x4& acc, const v4u& h, const v4u& o) {
#define MMA_(CH, X, Y) \
  if constexpr (FIRST) asm volatile("v_mfma_f32_16x16x32_bf16 %0, " X ", " Y ", 0" : "=&v"(acc) : CH(h), "v"(o)); \
  else asm volatile("v_mfma_f32_16x16x32_bf16 %0, " X ", " Y ", %0" : "+v"(acc) : CH(h), "v"(o));
  if constexpr (HA) { if constexpr (H_IS_A) { MMA_("a", "%1", "%2") } else { MMA_("a", "%2", "%1") } }
  else { if constexpr (H_IS_A) { MMA_("v", "%1", "%2") } else { MMA_("v", "%2", "%1") } }
#undef MMA_
}
DEV void mfma_settle() { asm volatile("s_nop 15" ::: "memory"); SB(); }

struct Ctx {
  char* ws; rsrc_t rs; unsigned lb; LAS char* lds; int lane, w; unsigned t0, t1;
  DEV v2u tag() const { return v2u{t0, t1}; }
};
DEV void flag_set(const Ctx& c, int f) { if (c.lane == 0) __builtin_amdgcn_raw_buffer_store_b64(c.tag(), c.rs, 0, O_FL + f * 8, SC1); }
DEV bool flag_ok(const Ctx& c, v2u v) { return __all(v[0] == c.t0 && v[1] == c.t1); }
DEV void flag_spin(const Ctx& c, int f) {
#pragma unroll 1
  for (int i = 0; i < 20000; ++i) {
    v2u v;
    asm volatile("buffer_load_dwordx2 %0, %1, %2, 0 offen offset:%3 sc1\n\ts_waitcnt vmcnt(0)" : "=&v"(v) : "v"(f * 8), "s"(c.rs), "i"(O_FL) : "memory");
    if (flag_ok(c, v)) return;
    __builtin_amdgcn_s_sleep(1);
  }
}
DEV void publish(const Ctx& c, int f) { waitv<0>(); bar(); if (c.w == 0) flag_set(c, f); }

constexpr int lo_of(int m, int n, int span) { int r = 0; while (r < n && (r * span) / n < m) ++r; return r; }
constexpr int mid_of(int m, int n, int nm) { int q = 0; while (q < n && ((2 * q + 1) * nm) / (2 * n) < m) ++q; return q; }
template <int NK, int NP, int NR, int NM, class Issue, class Rd, class Pin, class Mm>
DEV void raw_loop(Issue&& issue, Rd&& rd, Pin&& pinf, Mm&& mm) {
  constexpr int NS = KS / NK;
  sfor<3>([&](auto sc) { sfor<NP>([&](auto pc) { issue(sc, pc); }); });
  waitv<2 * NP>(); bar();
  sfor<NR>([&](auto rc) { rd(IC(0), rc); });
  SB();
  sfor<KS>([&](auto kc) {
    constexpr int k = CV(kc), s = k / NK, kk = k % NK;
    constexpr int p0 = (kk * NP) / NK, p1 = ((kk + 1) * NP) / NK, np = (s + 3 < NS) ? p1 - p0 : 0;
    if constexpr (kk == NK - 1 && s + 1 < NS) {
      waitlv<(s + 2 < NS ? NP : 0) + (s + 3 < NS ? p0 : 0)>(); bar();
    } else waitl();
    pinf(kc);
    SB();
    sfor<NM>([&](auto mc) {
      constexpr int m = CV(mc), span = NM - NM / 4;
      mm(kc, mc);
      SB();
      if constexpr (k + 1 < KS) srange<lo_of(m, NR, span), lo_of(m + 1, NR, span)>([&](auto rc) { rd(IC(k + 1), rc); SB(); });
      srange<mid_of(m, np, NM), mid_of(m + 1, np, NM)>([&](auto qc) { issue(IC(s + 3), IC(p0 + CV(qc))); SB(); });
    });
  });
}

DEV unsigned kc_rd(unsigned base, int lane, int h) { const int r = lane & 15, q = lane >> 4; return base + r * 128 + (((4 * h + q) ^ (r & 7)) << 4); }
template <bool PI> DEV int kc_src(int lane, int stride) { const int rr = lane >> 3; return (PI ? 8 * (rr >> 2) + (rr & 3) : rr) * stride + (((lane & 7) ^ rr) << 4); }
DEV int tr64_src(int lane, int w, int stride) { const int rr = lane >> 3; return rr * stride + (((lane & 7) ^ ((((rr >> 1) & 1) | ((w & 1) << 1)) << 1)) << 4); }
DEV unsigned tr64_rd(unsigned base, int lane, int w) {
  const int gg = lane >> 4, qq = (lane & 15) >> 2, pp = lane & 3;
  return base + (8 * gg + qq) * 128 + (((2 * w + (pp >> 1)) ^ (((qq >> 1) | ((gg & 1) << 1)) << 1)) << 4) + 8 * (pp & 1);
}
DEV int pi_inv_off(int t, int lane, int blk0, int blk1) {
  const int xx = 16 * (t & 1) + (lane & 15);
  return (((xx >> 2) & 1) ? blk1 : blk0) + (((lane >> 4) * 16 + 4 * (xx >> 3) + (xx & 3)) << 4);
}
DEV v4u cat(v2u a, v2u b) { return v4u{a[0], a[1], b[0], b[1]}; }

template <int TRB> DEV void w_tile(const Ctx& c, const us* wqkv, const us* wo, int ti, int tj) {
  const int lane = c.lane, w = c.w, wr = w >> 1, wc = w & 1;
  const rsrc_t ra = mkrs(wqkv, D * 3456 * 2), rb = TRB ? mkrs(wo, D * D * 2) : ra;
  const unsigned lb = c.lb;
  const int va = kc_src<true>(lane, 6912);
  const int sa = ti * 128 * 6912 + (TRB ? 4608 : 0) + (16 * (w & 1) + 4 * (w >> 1)) * 6912;
  int vb, sb;
  if constexpr (TRB) { vb = (lane >> 4) * 2304 + (((lane & 15) ^ (((lane >> 4) << 2) | w)) << 4); sb = tj * 256 + 4 * w * 2304; }
  else { vb = kc_src<false>(lane, 6912); sb = (tj * 128 + 8 * w) * 6912 + 2304; }
  unsigned ra_[2][2], rb_[2][8];
  sfor<2>([&](auto gc) { constexpr int g = CV(gc);
    sfor<2>([&](auto hc) { ra_[g][CV(hc)] = kc_rd(lb + g * 65536 + wr * 8192, lane, CV(hc)); });
    if constexpr (TRB) {
      const int gg = lane >> 4, qq = (lane & 15) >> 2, pp = lane & 3;
      sfor<8>([&](auto ec) { constexpr int cc = CV(ec) >> 1, t = CV(ec) & 1;
        const int row = 8 * gg + 4 * t + qq, ch = 2 * (4 * wc + cc) + (pp >> 1);
        rb_[g][CV(ec)] = lb + g * 65536 + 16384 + 256 * row + 16 * (ch ^ ((qq << 2) | ((2 * gg + t) & 3))) + 8 * (pp & 1); });
    } else sfor<2>([&](auto hc) { rb_[g][CV(hc)] = kc_rd(lb + g * 65536 + 16384 + wc * 8192, lane, CV(hc)); }); });
  v4u fa[2][4], fb[2][4];
  v2u ft[2][8];
  f32x4 acc[4][4];
  raw_loop<2, 8, TRB ? 12 : 8, 16>(
    [&](auto sc, auto pc) { constexpr int s = CV(sc), p = CV(pc), u = p >> 1;
      const unsigned m = lb + (s & 3) * 32768 + (p & 1) * 16384 + u * 4096 + w * 1024;
      if constexpr ((p & 1) == 0) dma_m0(m, va, ra, sa + u * 32 * 6912 + s * 128);
      else if constexpr (TRB) dma_m0(m, vb, rb, sb + (u * 16 + s * 64) * 2304);
      else dma_m0(m, vb, rb, sb + u * 32 * 6912 + s * 128); },
    [&](auto kc, auto rc) { constexpr int k = CV(kc), r = CV(rc), s = (k >> 1) & 3, h = k & 1;
      if constexpr (r < 4) dsr16<(s & 1) * 32768 + r * 2048>(fa[k & 1][r], ra_[s >> 1][h]);
      else if constexpr (TRB) dsrtr<(s & 1) * 32768 + h * 8192>(ft[k & 1][r - 4], rb_[s >> 1][r - 4]);
      else dsr16<(s & 1) * 32768 + (r - 4) * 2048>(fb[k & 1][r - 4], rb_[s >> 1][h]); },
    [&](auto kc) { constexpr int k = CV(kc);
      sfor<4>([&](auto ic) { pin(fa[k & 1][CV(ic)]); });
      if constexpr (TRB) sfor<4>([&](auto ic) { constexpr int i = CV(ic); pin(ft[k & 1][2 * i]); pin(ft[k & 1][2 * i + 1]); fb[k & 1][i] = cat(ft[k & 1][2 * i], ft[k & 1][2 * i + 1]); });
      else sfor<4>([&](auto ic) { pin(fb[k & 1][CV(ic)]); }); },
    [&](auto kc, auto mc) { constexpr int k = CV(kc), i = CV(mc) & 3, j = CV(mc) >> 2;
      acc[i][j] = mfma(fa[k & 1][i], fb[k & 1][j], k == 0 ? f32x4{0, 0, 0, 0} : acc[i][j]); });
  const int ks = ti * 4 + wr * 2;
  auto blk = [&](int T, int v) {
    if constexpr (TRB) return T < 60 ? (T / 5) * 324 + 144 + (ks + v) * 5 + T % 5 : (12 + (T - 60) / 3) * 324 + 216 + (ks + v) * 3 + (T - 60) % 3;
    else return T < 48 ? (T >> 2) * 324 + (ks + v) * 4 + (T & 3) : (12 + (T - 48) / 6) * 324 + (ks + v) * 6 + (T - 48) % 6; };
  sfor<4>([&](auto jc) { constexpr int j = CV(jc);
    const int T0 = tj * 8 + wc * 4 + (j & 2);
    sfor<2>([&](auto vc) { constexpr int v = CV(vc);
      st16(pack8(acc[2 * v][j], acc[2 * v + 1][j]), c.rs, pi_inv_off(j, lane, blk(T0, v) * 1024, blk(T0 + 1, v) * 1024), O_WT); }); });
  publish(c, F_W + (TRB * 9 + tj) * 9 + ti);
}

DEV void kvc_tile(const Ctx& c, const us* y, const us* wkv, int tn) {
  const int lane = c.lane, w = c.w;
  const rsrc_t r64 = mkrs(wkv, D * 2304 * 2), ry = mkrs(y, 154 * D * 2);
  const int v64 = tr64_src(lane, w, 4608), s64 = tn * 128 + 8 * w * 4608;
  const int vy = kc_src<false>(lane, D * 2);
  const int vyx = ((lane >> 3) < 5 || !(w & 1)) ? vy : 0x7ff00000;
  const int sy = ((w >> 1) * 77 + (w & 1) * 40) * (D * 2);
  unsigned r64a[2], rya[2][2];
  sfor<2>([&](auto gc) { constexpr int g = CV(gc); r64a[g] = tr64_rd(c.lb + g * 57344, lane, w);
    sfor<2>([&](auto hc) { rya[g][CV(hc)] = kc_rd(c.lb + g * 57344 + 8192, lane, CV(hc)); }); });
  v4u f64[2], fy[2][10];
  v2u ft[2][2];
  f32x4 acc[10];
  raw_loop<2, 7, 12, 10>(
    [&](auto sc, auto pc) { constexpr int s = CV(sc), p = CV(pc);
      const unsigned m = c.lb + (s & 3) * 28672;
      if constexpr (p < 2) dma_m0(m + (w + 4 * p) * 1024, v64, r64, s64 + (s * 64 + 32 * p) * 4608);
      else dma_m0(m + 8192 + (5 * w + p - 2) * 1024, p == 6 ? vyx : vy, ry, sy + (p - 2) * 8 * (D * 2) + s * 128); },
    [&](auto kc, auto rc) { constexpr int k = CV(kc), r = CV(rc), s = (k >> 1) & 3;
      if constexpr (r < 2) dsrtr<(s & 1) * 28672 + (k & 1) * 4096 + r * 512>(ft[k & 1][r], r64a[s >> 1]);
      else dsr16<(s & 1) * 28672 + (r - 2) * 2048>(fy[k & 1][r - 2], rya[s >> 1][k & 1]); },
    [&](auto kc) { constexpr int k = CV(kc);
      sfor<10>([&](auto ic) { pin(fy[k & 1][CV(ic)]); });
      pin(ft[k & 1][0]); pin(ft[k & 1][1]); f64[k & 1] = cat(ft[k & 1][0], ft[k & 1][1]); },
    [&](auto kc, auto mc) { constexpr int k = CV(kc), m = CV(mc); acc[m] = mfma(f64[k & 1], fy[k & 1][m], k == 0 ? f32x4{0, 0, 0, 0} : acc[m]); });
  const int isv = tn >= 18, n0 = (tn - 18 * isv) * 64 + 16 * w, q = lane >> 4;
  const int vo = ((2 * (w & 1) + (q >> 1)) * 16 + (lane & 15)) * 16 + (q & 1) * 8;
  sfor<10>([&](auto tc) { constexpr int t = CV(tc); st8(pack4(acc[t]), c.rs, vo, (isv ? O_VC : O_KC) + (((t / 5) * 36 + (n0 >> 5)) * 5 + t % 5) * 1024); });
  publish(c, F_KVC + tn);
}

template <int TR> DEV void cross_tile(const Ctx& c, const us* wgt, int b, int t) {
  const int lane = c.lane, w = c.w;
  const rsrc_t r64 = mkrs(wgt, D * D * 2);
  const int v64 = TR ? tr64_src(lane, w, D * 2) : kc_src<true>(lane, D * 2);
  const int s64 = TR ? t * 128 + 8 * w * (D * 2) : (t * 64 + 16 * (w & 1) + 4 * (w >> 1)) * (D * 2);
  const int s80 = (TR ? O_VC : O_KC) + b * (180 * 1024) + w * 5120;
  unsigned r64a[3][2], r80a[3];
  sfor<3>([&](auto gc) { constexpr int g = CV(gc); const unsigned base = c.lb + g * 49152;
    if constexpr (TR) r64a[g][0] = tr64_rd(base, lane, w); else sfor<2>([&](auto hc) { r64a[g][CV(hc)] = kc_rd(base + w * 2048, lane, CV(hc)); });
    r80a[g] = base + lane * 16; });
  v4u f64[2], f80[2][5];
  v2u ft[2][2];
  f32x4 acc[5];
  flag_spin(c, F_KVC + TR * 18 + (lane < 18 ? lane : 0));
  raw_loop<4, 9, TR ? 7 : 6, 5>(
    [&](auto sc, auto pc) { constexpr int s = CV(sc), p = CV(pc);
      const unsigned m = c.lb + (s & 3) * 36864;
      if constexpr (p < 4) {
        if constexpr (TR) dma_m0(m + (w + 4 * p) * 1024, v64, r64, s64 + (s * 128 + 32 * p) * (D * 2));
        else dma_m0(m + (p >> 1) * 8192 + (w + 4 * (p & 1)) * 1024, v64, r64, s64 + 32 * (p & 1) * (D * 2) + s * 256 + (p >> 1) * 128);
      } else dma_m0_h(m + 16384 + (5 * w + p - 4) * 1024, lane * 16, c.rs, s80 + s * 20480 + (p - 4) * 1024); },
    [&](auto kc, auto rc) { constexpr int k = CV(kc), r = CV(rc), kk = k & 3, so = ((k >> 2) & 3) * 36864;
      if constexpr (r < 5) { constexpr int o = so + 16384 + (kk * 5 + r) * 1024; dsr16<o % 49152>(f80[k & 1][r], r80a[o / 49152]); }
      else if constexpr (TR) { constexpr int o = so + kk * 4096 + (r - 5) * 512; dsrtr<o % 49152>(ft[k & 1][r - 5], r64a[o / 49152][0]); }
      else { constexpr int o = so + (kk >> 1) * 8192; dsr16<o % 49152>(f64[k & 1], r64a[o / 49152][kk & 1]); } },
    [&](auto kc) { constexpr int k = CV(kc);
      sfor<5>([&](auto ic) { pin(f80[k & 1][CV(ic)]); });
      if constexpr (TR) { pin(ft[k & 1][0]); pin(ft[k & 1][1]); f64[k & 1] = cat(ft[k & 1][0], ft[k & 1][1]); } else pin(f64[k & 1]); },
    [&](auto kc, auto mc) { constexpr int k = CV(kc), m = CV(mc);
      const f32x4 z = k == 0 ? f32x4{0, 0, 0, 0} : acc[m];
      acc[m] = TR ? mfma(f80[k & 1][m], f64[k & 1], z) : mfma(f64[k & 1], f80[k & 1][m], z); });
  if constexpr (TR) {
    const int T0 = b * 72 + t * 4 + (w & 2);
    const int off = pi_inv_off(w, lane, T0 * 3072, (T0 + 1) * 3072);
    st16(pack8(acc[0], acc[1]), c.rs, off, O_VPT);
    st16(pack8(acc[2], acc[3]), c.rs, off, O_VPT + 1024);
    st16(pack8(acc[4], f32x4{0, 0, 0, 0}), c.rs, off, O_VPT + 2048);
  } else {
    LAS float* red = (LAS float*)(c.lds + LDS_MISC);
    sfor<5>([&](auto tc) { const v2u v = pack4(acc[CV(tc)]);
      st8(v, c.rs, lane * 16 + (w & 1) * 8, O_WKT + ((b * 36 + 2 * t + (w >> 1)) * 5 + CV(tc)) * 1024);
      const float s = xsum16_32((bflo(v[0]) + bfhi(v[0])) + (bflo(v[1]) + bfhi(v[1])));
      if (lane < 16) red[w * 80 + CV(tc) * 16 + lane] = s; });
    lbar();
    const int tid = w * 64 + lane;
    if (tid < 80) __builtin_amdgcn_raw_buffer_store_b32(asu((red[tid] + red[80 + tid]) + (red[160 + tid] + red[240 + tid])), c.rs, tid * 4, O_U + (b * 18 + t) * 320, SC1);
  }
  publish(c, F_X + (TR * 2 + b) * 18 + t);
}

DEV void mod_strip(const Ctx& c, const us* cin, const us* wmod, int strip) {
  const int lane = c.lane, w = c.w, tid = w * 64 + lane;
  const rsrc_t rw = mkrs(wmod, D * 6912 * 2);
  float cv[9];
  for (int i = 0; i < 9; ++i) cv[i] = bflo(cin[tid + 256 * i]);
  LAS us* sc = (LAS us*)(c.lds + LDS_MISC);
  for (int i = 0; i < 9; ++i) sc[tid + 256 * i] = (us)pk2(cv[i] / (1.f + __expf(-cv[i])), 0.f);
  if (tid < 8) sc[2304 + tid] = 0;
  const int v64 = tr64_src(lane, w, 13824), s64 = strip * 128 + 8 * w * 13824;
  sfor<36>([&](auto pc) { dma_m0(c.lb + (w + 4 * CV(pc)) * 1024, v64, rw, s64 + CV(pc) * 32 * 13824); });
  const unsigned cb = c.lb + LDS_MISC + ((lane & 15) < 2 ? (lane & 15) * 2304 + (lane >> 4) * 16 : 4608), cs = (lane & 15) < 2 ? 64 : 0;
  f32x4 acc = {0, 0, 0, 0};
  sfor<3>([&](auto gc) { constexpr int g = CV(gc);
    waitlv<24 - 12 * g>(); bar();
    const unsigned ra = tr64_rd(c.lb + g * 49152, lane, w);
    v2u ft[12][2]; v4u cf[12];
    sfor<6>([&](auto kc) { constexpr int k = CV(kc); dsrtr<k * 4096>(ft[k][0], ra); dsrtr<k * 4096 + 512>(ft[k][1], ra); });
    sfor<6>([&](auto kc) { dsr16<0>(cf[CV(kc)], cb + (12 * g + CV(kc)) * cs); });
    waitl();
    srange<6, 12>([&](auto kc) { constexpr int k = CV(kc); dsrtr<k * 4096>(ft[k][0], ra); dsrtr<k * 4096 + 512>(ft[k][1], ra); });
    sfor<6>([&](auto kc) { constexpr int k = CV(kc); pin(ft[k][0]); pin(ft[k][1]); pin(cf[k]); acc = mfma(cat(ft[k][0], ft[k][1]), cf[k], acc); });
    srange<6, 12>([&](auto kc) { dsr16<0>(cf[CV(kc)], cb + (12 * g + CV(kc)) * cs); });
    waitl();
    srange<6, 12>([&](auto kc) { constexpr int k = CV(kc); pin(ft[k][0]); pin(ft[k][1]); pin(cf[k]); acc = mfma(cat(ft[k][0], ft[k][1]), cf[k], acc); }); });
  if ((lane & 15) < 2) st8(pack4(acc), c.rs, (lane & 15) * 6912 + (lane >> 4) * 8, O_MOD + strip * 128 + w * 32);
  publish(c, F_MOD + strip);
}

DEV void ln_tiles(const Ctx& c, const us* xin, int rt0) {
  const int lane = c.lane, w = c.w, r = lane & 15, q = lane >> 4;
  LAS float* red = (LAS float*)(c.lds + LDS_MISC + 8192);
  const rsrc_t rx = mkrs(xin, 2048 * D * 2);
  const int nt = rt0 < 12 ? 3 : 2;
  v4u xv[3][9];
  float mean[3], rstd[3];
  sfor<3>([&](auto nc) { sfor<9>([&](auto kc) { xv[CV(nc)][CV(kc)] = ld16(rx, r * (D * 2) + q * 16, (rt0 + 58 * CV(nc)) * (16 * D * 2) + (9 * w + CV(kc)) * 64); }); });
  sfor<3>([&](auto nc) { constexpr int n = CV(nc);
    if (n < nt) {
      float s = 0.f;
      sfor<9>([&](auto kc) { for (int e = 0; e < 4; ++e) s += bflo(xv[n][CV(kc)][e]) + bfhi(xv[n][CV(kc)][e]); });
      s = xsum16_32(s);
      if (lane < 16) red[n * 128 + w * 16 + lane] = s;
    }
    sfor<9>([&](auto kc) { pin(xv[n][CV(kc)]); }); SB(); });
  lbar();
  sfor<3>([&](auto nc) { constexpr int n = CV(nc);
    mean[n] = ((red[n * 128 + r] + red[n * 128 + 16 + r]) + (red[n * 128 + 32 + r] + red[n * 128 + 48 + r])) * (1.f / D);
    if (n < nt) {
      float s = 0.f;
      sfor<9>([&](auto kc) { for (int e = 0; e < 4; ++e) { const float d0 = bflo(xv[n][CV(kc)][e]) - mean[n], d1 = bfhi(xv[n][CV(kc)][e]) - mean[n]; s += d0 * d0 + d1 * d1; } });
      s = xsum16_32(s);
      if (lane < 16) red[n * 128 + 64 + w * 16 + lane] = s;
    }
    sfor<9>([&](auto kc) { pin(xv[n][CV(kc)]); }); SB(); });
  lbar();
  sfor<3>([&](auto nc) { constexpr int n = CV(nc);
    rstd[n] = __builtin_amdgcn_rsqf(((red[n * 128 + 64 + r] + red[n * 128 + 80 + r]) + (red[n * 128 + 96 + r] + red[n * 128 + 112 + r])) * (1.f / D) + 1e-5f); });
  flag_spin(c, F_MOD + (lane < 36 ? lane : 0));
  sfor<3>([&](auto nc) { constexpr int n = CV(nc);
    const int rt = rt0 + 58 * n;
    if (n < nt) {
      v4u sh[9], sc[9];
      sfor<9>([&](auto kc) { sh[CV(kc)] = ld16h(c.rs, q * 16, O_MOD + (rt >> 6) * 6912 + (9 * w + CV(kc)) * 64); sc[CV(kc)] = ld16h(c.rs, q * 16, O_MOD + (rt >> 6) * 6912 + 2304 + (9 * w + CV(kc)) * 64); });
      sfor<9>([&](auto kc) { constexpr int k = CV(kc);
        v4u o;
        for (int e = 0; e < 4; ++e)
          o[e] = pk2((bflo(xv[n][k][e]) - mean[n]) * rstd[n] * (1.f + bflo(sc[k][e])) + bflo(sh[k][e]), (bfhi(xv[n][k][e]) - mean[n]) * rstd[n] * (1.f + bfhi(sc[k][e])) + bfhi(sh[k][e]));
        st16(o, c.rs, lane * 16, O_H + (((rt >> 1) * 36 + 9 * w + k) * 2 + (rt & 1)) * 1024); SB(); });
    } });
  waitv<0>(); bar();
  if (w == 0 && lane < nt) __builtin_amdgcn_raw_buffer_store_b64(c.tag(), c.rs, lane * 58 * 8, O_FL + (F_H + rt0) * 8, SC1);
}

enum { OP_OCT, OP_A, OP_POLL, OP_ST };
struct Plan {
  static constexpr int MAXO = 200, MAXK = 110;
  int n = 0, kind[MAXO] = {}, idx[MAXO] = {}, wt[MAXO] = {}, need[MAXO] = {};
  int in0[MAXK] = {}, in1[MAXK] = {}, cnt[MAXK] = {}, pre = 0, cnt_pre = 0;
  bool ok = true;
  constexpr void add(int k, int i, int w, int nd) { kind[n] = k; idx[n] = i; wt[n] = w; need[n] = nd; ++n; }
  constexpr int left(int upto, int t) const {
    int last = -1, s = 0;
    for (int i = 0; i < upto; ++i) if (need[i] <= t) last = i;
    for (int i = last + 1; i < upto; ++i) s += wt[i];
    return s;
  }
};
template <int NT> struct P1 {
  static constexpr int NV = 9 - NT, NK = 108, NOCT = 59;
  static constexpr int G_W2 = 8, G_T1 = NT == 4 ? 65 : 61, G_T2 = 80, G_V = 108, FL_T = NT == 4 ? 44 : 48, FL_V = 80;
  static constexpr int nb(int k) { return k < 36 ? NT : k < 72 ? NV : 4; }
  static constexpr int piece(int k, int j) { return k < 36 ? k * NT + j : k < 72 ? 36 * NT + (k - 36) * NV + j : 328 + (k - 72) * 4 + j; }
  static constexpr int kof(int p) { return p < 36 * NT ? p / NT : p < 324 ? 36 + (p - 36 * NT) / NV : p < 328 ? 71 : 72 + (p - 328) / 4; }
  static constexpr bool isbar(int k) { return k % 4 == 0 || k < 4 || (k >= 70 && k <= 75); }
  static constexpr Plan make() {
    Plan p;
    auto need = [](int o) { int k = kof(8 * o) - 1; while (k >= 0 && !isbar(k)) --k; return k; };
    auto gate = [](int o) { return o >= 53 ? G_T2 : o >= 41 ? G_T1 : 8 * o + 7 >= 36 * NT ? G_W2 : 0; };
    int o = 0;
    for (; o < 3; ++o) p.add(OP_OCT, o, 2, need(o));
    p.pre = p.n; p.cnt_pre = p.left(p.n, -1);
    for (int k = 0; k < NK; ++k) {
      if (k == 36) p.add(OP_ST, 0, NT, FL_T);
      if (k == 72) p.add(OP_ST, 1, NV, FL_V);
      p.cnt[k] = p.left(p.n, k);
      p.in0[k] = p.n;
      if (k == G_W2 - 4) p.add(OP_POLL, 0, 1, G_W2);
      if (k == G_T1 - 5) p.add(OP_POLL, 1, 1, G_T1);
      if (k == G_T2 - 5) p.add(OP_POLL, 2, 1, G_T2);
      if (k == G_V - 6) p.add(OP_POLL, 3, 1, G_V);
      if (o < NOCT && gate(o) <= k) {
        bool free_ = o < 18;
        if (!free_) for (int kb = kof(8 * (o - 18) + 7); kb <= k; ++kb) if (isbar(kb)) free_ = true;
        if (free_) { if (need(o) <= k) p.ok = false; p.add(OP_OCT, o, 2, need(o)); ++o; }
      }
      p.in1[k] = p.n;
      if (p.cnt[k] > 60) p.ok = false;
    }
    if (o != NOCT) p.ok = false;
    return p;
  }
};
struct P3 {
  static constexpr int NK = 32, DIST = 5, AW = 7;
  static constexpr bool isbar(int k) { return k % 2 == 1; }
  static constexpr Plan make() {
    Plan p;
    for (int k = 0; k < DIST; ++k) p.add(OP_A, k, 1, k);
    p.pre = p.n;
    int o = 18;
    for (int k = 0; k < NK; ++k) {
      p.cnt[k] = p.left(p.n, k);
      p.in0[k] = p.n;
      if (k + DIST < NK) p.add(OP_A, k + DIST, 1, k + DIST);
      if (o < 40) {
        bool free_ = false;
        for (int kb = (8 * (o - 18) + 7) / 9; kb <= k; ++kb) if (isbar(kb)) free_ = true;
        int nd = o < 36 ? (8 * o) / 9 - 1 : NK;
        while (nd < NK && !isbar(nd)) --nd;
        if (free_) { if (nd <= k) p.ok = false; p.add(OP_OCT, o, 2, nd); ++o; }
      }
      p.in1[k] = p.n;
    }
    if (o != 40) p.ok = false;
    return p;
  }
};

template <int PAR> DEV void k3_epi(const Ctx& c, f32x4 (&o)[9], v4u (&xv)[5], v4u (&gv)[5], int nb, int qt) {
  const int lane = c.lane, vo = lane * 16, ks0 = (9 * nb) >> 1;
  v4u wk[5][5];
  sfor<5>([&](auto uc) { sfor<5>([&](auto tc) { dsr16<(CV(uc) * 5 + CV(tc)) * 1024>(wk[CV(uc)][CV(tc)], c.lb + vo); }); if constexpr (CV(uc) == 2) waitl(); });
  v4u x2[5];
  float s1 = 0.f;
  sfor<5>([&](auto uc) { constexpr int u = CV(uc);
    constexpr int tl = PAR ? 2 * u - 1 : 2 * u, th = tl + 1;
    x2[u] = v4u{0, 0, 0, 0};
    if constexpr (tl >= 0) { x2[u][0] = pk2(bflo(xv[u][0]) + bflo(gv[u][0]) * o[tl][0], bfhi(xv[u][0]) + bfhi(gv[u][0]) * o[tl][1]);
                             x2[u][1] = pk2(bflo(xv[u][1]) + bflo(gv[u][1]) * o[tl][2], bfhi(xv[u][1]) + bfhi(gv[u][1]) * o[tl][3]); }
    if constexpr (th <= 8) { x2[u][2] = pk2(bflo(xv[u][2]) + bflo(gv[u][2]) * o[th][0], bfhi(xv[u][2]) + bfhi(gv[u][2]) * o[th][1]);
                             x2[u][3] = pk2(bflo(xv[u][3]) + bflo(gv[u][3]) * o[th][2], bfhi(xv[u][3]) + bfhi(gv[u][3]) * o[th][3]); }
    for (int e = 0; e < 4; ++e) s1 += bflo(x2[u][e]) + bfhi(x2[u][e]);
    const int so = O_X2 + (qt * 36 + ks0 + u) * 1024;
    if constexpr (tl >= 0 && th <= 8) st16(x2[u], c.rs, vo, so);
    else if constexpr (tl >= 0) st8(v2u{x2[u][0], x2[u][1]}, c.rs, vo, so);
    else st8(v2u{x2[u][2], x2[u][3]}, c.rs, vo + 8, so); });
  waitl();
  sfor<5>([&](auto uc) { sfor<5>([&](auto tc) { pin(wk[CV(uc)][CV(tc)]); }); });
  sfor<5>([&](auto tc) { constexpr int t = CV(tc);
    f32x4 s = {0, 0, 0, 0};
    sfor<5>([&](auto uc) { s = mfma(wk[CV(uc)][t], x2[CV(uc)], s); });
    st16(__builtin_bit_cast(v4u, s), c.rs, vo, O_SCP + ((qt * 8 + nb) * 5 + t) * 1024); });
  const float mean = xsum16_32(s1) * (1.f / 144.f);
  float s2 = 0.f;
  sfor<5>([&](auto uc) { constexpr int u = CV(uc); constexpr int tl = PAR ? 2 * u - 1 : 2 * u, th = tl + 1;
    for (int e = (tl >= 0 ? 0 : 2); e < (th <= 8 ? 4 : 2); ++e) { const float d0 = bflo(x2[u][e]) - mean, d1 = bfhi(x2[u][e]) - mean; s2 += d0 * d0 + d1 * d1; } });
  s2 = xsum16_32(s2);
  if (lane < 16) st8(v2u{asu(mean), asu(s2)}, c.rs, lane * 8, O_LNP + (qt * 8 + nb) * 128);
}

template <int PAR> DEV void k4_out(v4u (&vp)[9][3], v4u (&pc)[3], v4u (&x2)[5], rsrc_t ro, int nb, int qt, int lane) {
  const int r = lane & 15, q = lane >> 4, ks0 = (9 * nb) >> 1;
  f32x4 o[9];
  sfor<9>([&](auto tc) { constexpr int t = CV(tc); o[t] = mfma(vp[t][2], pc[2], mfma(vp[t][1], pc[1], mfma(vp[t][0], pc[0], f32x4{0, 0, 0, 0}))); });
  const int oo = (qt * 16 + r) * (D * 2) + ks0 * 64 + q * 16;
  sfor<5>([&](auto uc) { constexpr int u = CV(uc); constexpr int tl = PAR ? 2 * u - 1 : 2 * u, th = tl + 1;
    v4u y = {0, 0, 0, 0};
    if constexpr (tl >= 0) { y[0] = pk2(bflo(x2[u][0]) + o[tl][0], bfhi(x2[u][0]) + o[tl][1]); y[1] = pk2(bflo(x2[u][1]) + o[tl][2], bfhi(x2[u][1]) + o[tl][3]); }
    if constexpr (th <= 8) { y[2] = pk2(bflo(x2[u][2]) + o[th][0], bfhi(x2[u][2]) + o[th][1]); y[3] = pk2(bflo(x2[u][3]) + o[th][2], bfhi(x2[u][3]) + o[th][3]); }
    if constexpr (tl >= 0 && th <= 8) __builtin_amdgcn_raw_buffer_store_b128(y, ro, oo, u * 64, 0);
    else if constexpr (tl >= 0) __builtin_amdgcn_raw_buffer_store_b64(v2u{y[0], y[1]}, ro, oo, u * 64, 0);
    else __builtin_amdgcn_raw_buffer_store_b64(v2u{y[2], y[3]}, ro, oo + 8, u * 64, 0); });
}

template <int NT> DEV void tv_scores(const Ctx& c, int mi, int ci, f32x4 (&sacc)[2][4], v2u& pollv) {
  using P = P1<NT>;
  constexpr int NV = 9 - NT;
  constexpr Plan pl = P::make();
  static_assert(pl.ok, "schedule");
  const int lane = c.lane, w = c.w, vo = lane * 16, b = mi >> 3, qb = b * 16 + ci;
  v4u h[72];
  flag_spin(c, F_H + mi * 8 + 2 * w + (lane & 1));
  { const int hb = O_H + (mi * 4 + w) * (72 * 1024);
    sfor<72>([&](auto fc) { constexpr int f = CV(fc);
      if constexpr (f < 64) ald16a<(f & 3) * 1024>(h[f], vo, c.rs, hb + (f >> 2) * 4096); else ald16<(f & 3) * 1024>(h[f], vo, c.rs, hb + (f >> 2) * 4096);
      if constexpr (f == 55) waitv<32>(); }); }
  STAMP(1, 1);
  const int t0 = NT == 4 ? ci * 4 : 48 + (ci - 12) * 6, v0 = NT == 4 ? ci * 5 : 60 + (ci - 12) * 3;
  const int l9 = lane < 18 ? lane : 0;
  const int fw1 = F_W + ((l9 < 9 ? t0 : t0 + NT - 1) >> 3) * 9 + l9 % 9;
  int pf[4];
  pf[0] = F_W + 81 + ((l9 < 9 ? v0 : v0 + NV - 1) >> 3) * 9 + l9 % 9;
  pf[1] = F_T + (qb >> 1) * 16 + (lane < 12 ? lane : 0);
  pf[2] = F_T + (qb >> 1) * 16 + 12 + (lane & 3);
  { const int nb = mi & 7, s0 = nb < 7 ? (9 * nb) / 5 : 13;
    pf[3] = lane < 24 ? F_V + (b * 16 + s0 + (lane >> 3)) * 8 + (lane & 7) : lane < 42 ? F_X + b * 18 + lane - 24 : lane < 60 ? F_MOD + 36 + lane - 42 : F_MOD + 36; }
  const int sw = O_WT + ci * (324 * 1024) + w * 2048, st = O_T + qb * (144 * 1024) + w * 2048 - 41 * 8192;
  const unsigned mw = c.lb + w * 2048;
  unsigned rd[3];
  rd[0] = c.lb + vo; rd[1] = rd[0] + 49152; rd[2] = rd[0] + 98304;
  v4u bf[2][6];
  auto op = [&](auto oc) { constexpr int kind = pl.kind[CV(oc)], idx = pl.idx[CV(oc)];
    if constexpr (kind == OP_OCT) { const int so = (idx < 41 ? sw : st) + idx * 8192; dma_m0_h(mw + ((8 * idx) % 144) * 1024, vo, c.rs, so); dma_2nd_h(vo, c.rs, so); }
    else if constexpr (kind == OP_POLL) ald8<O_FL>(pollv, pf[idx] * 8, c.rs, 0); };
  auto rdb = [&](auto kc, auto jc) { constexpr int p = P::piece(CV(kc), CV(jc)) % 144; dsr16<(p % 48) * 1024>(bf[CV(kc) & 1][CV(jc)], rd[p / 48]); };
  flag_spin(c, fw1);
  STAMP(1, 2);
  sfor<pl.pre>(op);
  waitv<pl.cnt_pre>(); bar();
  sfor<NT>([&](auto jc) { rdb(IC(0), jc); });
  SB();
  f32x4 acc[2][6];
  sfor<108>([&](auto kc) {
    constexpr int k = CV(kc), ks = k % 36, NB = P::nb(k), NM = 2 * NB;
    if constexpr (k == 36) {
      STAMP(1, 3);
      mfma_settle();
      const int tb = O_T + (mi * 2 + (w >> 1)) * (144 * 1024) + (w & 1) * 2048 + (t0 >> 1) * 4096;
      sfor<2>([&](auto ic) { sfor<NT / 2>([&](auto uc) { constexpr int i = CV(ic), u = CV(uc); st16(pack8(acc[i][2 * u], acc[i][2 * u + 1]), c.rs, vo, tb + u * 4096 + i * 1024); }); });
      asm volatile("s_nop 1"); SB();
    }
    if constexpr (k == 72) {
      STAMP(1, 4);
      mfma_settle();
      sfor<NV>([&](auto jc) { constexpr int j = CV(jc); const int T = v0 + j, nb = T / 9;
        st16(pack8(acc[0][j], acc[1][j]), c.rs, vo, O_VT + (((b * 8 + nb) * 32 + (mi & 7) * 4 + w) * 9 + T - 9 * nb) * 1024); });
      asm volatile("s_nop 1"); SB();
    }
    waitlv<pl.cnt[k]>();
    if constexpr (P::isbar(k)) bar();
    if constexpr (k == P::G_W2 || k == P::G_T1 || k == P::G_T2) {
      pin(pollv);
      if (!flag_ok(c, pollv)) flag_spin(c, pf[k == P::G_W2 ? 0 : k == P::G_T1 ? 1 : 2]);
    }
    if constexpr (k == P::FL_T) { if (w == 0) flag_set(c, F_T + mi * 16 + ci); }
    if constexpr (k == P::FL_V) { if (w == 0) flag_set(c, F_V + (b * 16 + ci) * 8 + (mi & 7)); }
    sfor<NB>([&](auto jc) { pin(bf[k & 1][CV(jc)]); });
    SB();
    sfor<NM>([&](auto mc) {
      constexpr int m = CV(mc), j = m >> 1, i = m & 1, f = 2 * ks + i, no = pl.in1[k] - pl.in0[k];
      if constexpr (k < 72) mma_h<(f < 64), (k >= 36), ks == 0>(acc[i][j], h[f], bf[k & 1][j]);
      else mma_h<(f < 64), true, ks == 0>(sacc[i][j], h[f], bf[k & 1][j]);
      SB();
      if constexpr (k + 1 < 108) { constexpr int NR = P::nb(k + 1), span = NM - NM / 4;
        srange<lo_of(m, NR, span), lo_of(m + 1, NR, span)>([&](auto rc) { rdb(IC(k + 1), rc); SB(); }); }
      srange<mid_of(m, no, NM), mid_of(m + 1, no, NM)>([&](auto qc) { op(IC(pl.in0[k] + CV(qc))); SB(); });
    });
  });
  mfma_settle();
  waitv<0>();
  pin(pollv);
  if (!flag_ok(c, pollv)) flag_spin(c, pf[3]);
}

__global__ __launch_bounds__(256, 1) void fused(char* ws, const us* xin, const us* cin, const us* y, const us* wqkv, const us* wo, const us* wq_c,
                                                const us* wkv, const us* wo_c, const us* wmod, us* out) {
  extern __shared__ __attribute__((aligned(16))) char smem[];
  Ctx c;
  c.ws = ws; c.rs = mkrs(ws, WS_BYTES); c.lds = (LAS char*)smem; c.lb = (unsigned)(size_t)c.lds;
  c.lane = threadIdx.x & 63; c.w = __builtin_amdgcn_readfirstlane(threadIdx.x >> 6);
  const int lane = c.lane, w = c.w, vo = lane * 16;
  STAMP(0, 0);
  {
    const v2u e = __builtin_amdgcn_raw_buffer_load_b64(c.rs, 0, O_EP, SC1);
    const unsigned long long t = (((unsigned long long)(unsigned)__builtin_amdgcn_readfirstlane(e[1]) << 32) | (unsigned)__builtin_amdgcn_readfirstlane(e[0])) + 0x9E3779B97F4A7C15ull;
    c.t0 = (unsigned)t; c.t1 = (unsigned)(t >> 32);
  }
  const int x = blockIdx.x & 7, l = blockIdx.x >> 3;
  {
    const int ni = (x & 1) ? 4 : 5, nj = (x & 2) ? 4 : 5, nw = ni * nj;
    if (l < nw) {
      const int ti = (x & 1) * 5 + l % ni, tj = ((x >> 1) & 1) * 5 + l / ni;
      if (x < 4) w_tile<0>(c, wqkv, wo, ti, tj); else w_tile<1>(c, wqkv, wo, ti, tj);
    } else {
      const int e = l - nw, q = x & 3, nk = q + 3;
      int ct;
      if (e < nk) {
        ct = (x >> 2) * 18 + (q == 0 ? 0 : q == 1 ? 3 : q == 2 ? 7 : 12) + e;
        kvc_tile(c, y, wkv, ct);
      } else {
        const int aa = ((q == 0 ? 0 : q == 1 ? 4 : q == 2 ? 12 : 19) + e - nk) * 2 + (x >> 2);
        if (aa < 54) mod_strip(c, cin, wmod, aa);
        STAMP(0, 1);
        ln_tiles(c, xin, 57 - aa);
        ct = aa < 36 ? 36 + aa : -1;
      }
      STAMP(0, 2);
      ct = __builtin_amdgcn_readfirstlane(ct);
      if (ct >= 36) cross_tile<1>(c, wo_c, ct & 1, (ct - 36) >> 1);
      else if (ct >= 0) cross_tile<0>(c, wq_c, ct & 1, ct >> 1);
    }
  }
  STAMP(1, 0);
  const int mi = (x & 1) * 8 + (l & 7), ci = (x >> 1) * 4 + (l >> 3), b = mi >> 3, kb = mi & 7, qb = b * 16 + ci;
  f32x4 sacc[2][4];
  v2u pollv;
  if (ci < 12) tv_scores<4>(c, mi, ci, sacc, pollv); else tv_scores<6>(c, mi, ci, sacc, pollv);
  STAMP(1, 5);
  const int nb = kb, qt = qb * 4 + w;
  const int sv = O_VT + (b * 8 + nb) * (288 * 1024) + w * 2048;
  const unsigned mw = c.lb + w * 2048;
  {
    LAS float* red = (LAS float*)(c.lds + LDS_MISC);
    const float cs = 0.029462782549439476f * 1.4426950408889634f;
    const int q = lane >> 4;
    float mx[4], sm[4];
    sfor<4>([&](auto jc) { constexpr int j = CV(jc);
      mx[j] = xmax16_32(fmaxf(fmaxf(fmaxf(sacc[0][j][0], sacc[0][j][1]), fmaxf(sacc[0][j][2], sacc[0][j][3])), fmaxf(fmaxf(sacc[1][j][0], sacc[1][j][1]), fmaxf(sacc[1][j][2], sacc[1][j][3])))); });
    red[w * 64 + lane] = q == 0 ? mx[0] : q == 1 ? mx[1] : q == 2 ? mx[2] : mx[3];
    lbar();
    sfor<4>([&](auto jc) { constexpr int j = CV(jc);
      const int o = j * 16 + (lane & 15);
      const float m = fmaxf(fmaxf(red[o], red[64 + o]), fmaxf(red[128 + o], red[192 + o])) * cs;
      mx[j] = m;
      f32x4 p0, p1;
      for (int r = 0; r < 4; ++r) { p0[r] = __builtin_amdgcn_exp2f(sacc[0][j][r] * cs - m); p1[r] = __builtin_amdgcn_exp2f(sacc[1][j][r] * cs - m); }
      st16(pack8(p0, p1), c.rs, vo, O_P + ((qb * 4 + j) * 32 + kb * 4 + w) * 1024);
      sm[j] = xsum16_32(((p0[0] + p0[1]) + (p0[2] + p0[3])) + ((p1[0] + p1[1]) + (p1[2] + p1[3]))); });
    st8(v2u{asu(q == 0 ? mx[0] : q == 1 ? mx[1] : q == 2 ? mx[2] : mx[3]), asu(q == 0 ? sm[0] : q == 1 ? sm[1] : q == 2 ? sm[2] : sm[3])}, c.rs,
        q * 4096 + (lane & 15) * 8, O_ST + qb * 16384 + kb * 512 + w * 128);
    SB();
  }
  sfor<8>([&](auto oc) { dma_m0_h(mw + CV(oc) * 8192, vo, c.rs, sv + CV(oc) * 8192); dma_2nd_h(vo, c.rs, sv + CV(oc) * 8192); });
  waitv<16>();
  flag_set(c, F_P + qb * 32 + kb * 4 + w);
  STAMP(1, 6);
  v4u xv[5], gv[5];
  { const rsrc_t rx = mkrs(xin, 2048 * D * 2);
    const int ks0 = (9 * nb) >> 1;
    sfor<5>([&](auto uc) { ald16<CV(uc) * 64>(xv[CV(uc)], (lane & 15) * (D * 2) + (lane >> 4) * 16, rx, qt * (16 * D * 2) + ks0 * 64);
                           ald16<CV(uc) * 64>(gv[CV(uc)], (lane >> 4) * 16, c.rs, O_MOD + (b * 3456 + 2304) * 2 + ks0 * 64); }); }
  srange<8, 18>([&](auto oc) { dma_m0_h(mw + CV(oc) * 8192, vo, c.rs, sv + CV(oc) * 8192); dma_2nd_h(vo, c.rs, sv + CV(oc) * 8192); });
  flag_spin(c, F_P + qb * 32 + (lane & 31));
  STAMP(1, 7);
  f32x4 tot[9];
  {
    constexpr Plan pl = P3::make();
    static_assert(pl.ok, "schedule");
    v2u sts[8];
    sfor<8>([&](auto ic) { ald8<CV(ic) * 128>(sts[CV(ic)], (lane >> 4) * 1024 + (lane & 15) * 8, c.rs, O_ST + qt * 4096); });
    const int sk = O_WKT + (b * 36 + ((9 * nb) >> 1)) * 5120 + w * 2048 - 36 * 8192, ab = O_P + qt * 32768;
    unsigned rd[3];
    rd[0] = c.lb + vo; rd[1] = rd[0] + 49152; rd[2] = rd[0] + 98304;
    v4u a[P3::AW], bf[2][9];
    f32x4 acc[9];
    float f[8];
    auto op = [&](auto oc) { constexpr int kind = pl.kind[CV(oc)], idx = pl.idx[CV(oc)];
      if constexpr (kind == OP_OCT) { const int so = (idx < 36 ? sv : sk) + idx * 8192; dma_m0_h(mw + ((8 * idx) % 144) * 1024, vo, c.rs, so); dma_2nd_h(vo, c.rs, so); }
      else ald16<(idx & 3) * 1024>(a[idx % P3::AW], vo, c.rs, ab + (idx >> 2) * 4096); };
    auto rdb = [&](auto kc, auto jc) { constexpr int p = (CV(kc) * 9 + CV(jc)) % 144; dsr16<(p % 48) * 1024>(bf[CV(kc) & 1][CV(jc)], rd[p / 48]); };
    sfor<pl.pre>(op);
    bar();
    sfor<9>([&](auto jc) { rdb(IC(0), jc); });
    SB();
    sfor<32>([&](auto kc) {
      constexpr int k = CV(kc), no = pl.in1[k] - pl.in0[k];
      waitlv<pl.cnt[k]>();
      if constexpr (P3::isbar(k)) bar();
      sfor<9>([&](auto jc) { pin(bf[k & 1][CV(jc)]); });
      pin(a[k % P3::AW]);
      if constexpr (k == 1) {
        sfor<8>([&](auto ic) { pin(sts[CV(ic)]); });
        const float m0 = asf(sts[0][0]), m1 = asf(sts[4][0]), m = xmax16_32(fmaxf(m0, m1));
        const float e0 = __builtin_amdgcn_exp2f(m0 - m), e1 = __builtin_amdgcn_exp2f(m1 - m);
        const float s = __builtin_amdgcn_rcpf(xsum16_32(e0 * ((asf(sts[0][1]) + asf(sts[1][1])) + (asf(sts[2][1]) + asf(sts[3][1]))) + e1 * ((asf(sts[4][1]) + asf(sts[5][1])) + (asf(sts[6][1]) + asf(sts[7][1])))));
        for (int i = 0; i < 4; ++i) { f[2 * i] = __shfl(e0 * s, (lane & 15) + 16 * i); f[2 * i + 1] = __shfl(e1 * s, (lane & 15) + 16 * i); }
        waitl();
      }
      SB();
      sfor<9>([&](auto mc) {
        constexpr int m = CV(mc);
        if constexpr (k % 4 == 0 && k > 0) { if constexpr (k == 4) tot[m] = acc[m] * f[0]; else tot[m] += acc[m] * f[k / 4 - 1]; }
        acc[m] = mfma(bf[k & 1][m], a[k % P3::AW], k % 4 == 0 ? f32x4{0, 0, 0, 0} : acc[m]);
        SB();
        if constexpr (k + 1 < 32) srange<lo_of(m, 9, 7), lo_of(m + 1, 9, 7)>([&](auto rc) { rdb(IC(k + 1), rc); SB(); });
        srange<mid_of(m, no, 9), mid_of(m + 1, no, 9)>([&](auto qc) { op(IC(pl.in0[k] + CV(qc))); SB(); });
      });
    });
    sfor<9>([&](auto jc) { tot[CV(jc)] += acc[CV(jc)] * f[7]; });
  }
  STAMP(1, 8);
  waitv<0>(); bar();
  sfor<5>([&](auto uc) { pin(xv[CV(uc)]); pin(gv[CV(uc)]); });
  if (nb & 1) k3_epi<1>(c, tot, xv, gv, nb, qt); else k3_epi<0>(c, tot, xv, gv, nb, qt);
  const int qt4 = qb * 4 + (nb >> 1), nb4 = (nb & 1) * 4 + w;
  SB();
  v4u vp[9][3];
  flag_spin(c, F_X + 36 + b * 18 + (lane < 18 ? lane : 0));
  flag_set(c, F_S + qt * 8 + nb);
  STAMP(1, 9);
  sfor<27>([&](auto ic) { constexpr int i = CV(ic); ald16<(i & 3) * 1024>(vp[i / 3][i % 3], vo, c.rs, O_VPT + (b * 72 + 9 * nb4) * 3072 + (i >> 2) * 4096); });
  LAS float* ured = (LAS float*)(c.lds + LDS_MISC + 2048);
  { const int tid = w * 64 + lane;
    if (tid < 240) { const int g = tid / 80, m = tid % 80; float u = 0.f;
      for (int i = 0; i < 6; ++i) u += asf(__builtin_amdgcn_raw_buffer_load_b32(c.rs, (g * 6 + i) * 320 + m * 4, O_U + b * 5760, SC1));
      ured[tid] = u; } }
  if (blockIdx.x == 0 && w == 3) {
    sfor<4>([&](auto ic) { flag_spin(c, F_T + CV(ic) * 64 + lane); });
    if (lane == 0) __builtin_amdgcn_raw_buffer_store_b64(c.tag(), c.rs, 0, O_EP, SC1);
  }
  flag_spin(c, F_S + qt4 * 8 + (lane & 7));
  STAMP(1, 10);
  {
    const int r = lane & 15, q = lane >> 4;
    v4u sp[2][5], x2[5];
    v2u lp[2];
    sfor<10>([&](auto ic) { constexpr int i = CV(ic); ald16<(i & 3) * 1024>(sp[i / 5][i % 5], vo, c.rs, O_SCP + (qt4 * 8 + 2 * w) * 5120 + (i >> 2) * 4096); });
    sfor<2>([&](auto ic) { ald8<CV(ic) * 128>(lp[CV(ic)], q * 256 + r * 8, c.rs, O_LNP + qt4 * 1024); });
    sfor<5>([&](auto uc) { constexpr int u = CV(uc); ald16<(u & 3) * 1024>(x2[u], vo, c.rs, O_X2 + (qt4 * 36 + ((9 * nb4) >> 1)) * 1024 + (u >> 2) * 4096); });
    waitv<5>();
    sfor<10>([&](auto ic) { pin(sp[CV(ic) / 5][CV(ic) % 5]); });
    pin(lp[0]); pin(lp[1]);
    LAS f32x4* red = (LAS f32x4*)(c.lds + 32768);
    sfor<5>([&](auto tc) { constexpr int t = CV(tc); red[(w * 5 + t) * 64 + lane] = __builtin_bit_cast(f32x4, sp[0][t]) + __builtin_bit_cast(f32x4, sp[1][t]); });
    const float mean = xsum16_32(asf(lp[0][0]) + asf(lp[1][0])) * 0.125f;
    const float d0 = asf(lp[0][0]) - mean, d1 = asf(lp[1][0]) - mean;
    const float m2 = xsum16_32(asf(lp[0][1]) + asf(lp[1][1]) + 144.f * (d0 * d0 + d1 * d1));
    const float cc = 0.029462782549439476f * 1.4426950408889634f * __builtin_amdgcn_rsqf(m2 * (1.f / D) + 1e-5f);
    lbar();
    f32x4 s[5];
    float mx = -3.0e38f;
    sfor<5>([&](auto tc) { constexpr int t = CV(tc);
      s[t] = (red[t * 64 + lane] + red[(5 + t) * 64 + lane]) + (red[(10 + t) * 64 + lane] + red[(15 + t) * 64 + lane]);
      const LAS f32x4* u4 = (const LAS f32x4*)(ured + t * 16 + q * 4);
      const f32x4 u = (u4[0] + u4[20]) + u4[40];
      for (int e = 0; e < 4; ++e) {
        s[t][e] = (t * 16 + q * 4 + e < 77) ? (s[t][e] - mean * u[e]) * cc : -3.0e38f;
        mx = fmaxf(mx, s[t][e]);
      } });
    mx = xmax16_32(mx);
    float sum = 0.f;
    sfor<5>([&](auto tc) { constexpr int t = CV(tc); for (int e = 0; e < 4; ++e) { s[t][e] = __builtin_amdgcn_exp2f(s[t][e] - mx); sum += s[t][e]; } });
    sum = __builtin_amdgcn_rcpf(xsum16_32(sum));
    v4u pc[3];
    pc[0] = pack8(s[0] * sum, s[1] * sum); pc[1] = pack8(s[2] * sum, s[3] * sum); pc[2] = pack8(s[4] * sum, f32x4{0, 0, 0, 0});
    waitv<0>();
    sfor<27>([&](auto ic) { pin(vp[CV(ic) / 3][CV(ic) % 3]); });
    sfor<5>([&](auto uc) { pin(x2[CV(uc)]); });
    const rsrc_t ro = mkrs(out, 2048 * D * 2);
    if (w & 1) k4_out<1>(vp, pc, x2, ro, nb4, qt4, lane); else k4_out<0>(vp, pc, x2, ro, nb4, qt4, lane);
  }
}
}

extern "C" void kernel_launch(void* const* d_in, const int* in_sizes, int n_in, void* d_out, int out_size, void* d_ws, size_t ws_size, hipStream_t stream) {
  (void)hipFuncSetAttribute((const void*)fused, hipFuncAttributeMaxDynamicSharedMemorySize, LDS_BYTES);
  fused<<<256, 256, LDS_BYTES, stream>>>((char*)d_ws, (const us*)d_in[0], (const us*)d_in[1], (const us*)d_in[2], (const us*)d_in[3], (const us*)d_in[4],
                                         (const us*)d_in[5], (const us*)d_in[6], (const us*)d_in[7], (const us*)d_in[8], (us*)d_out);
}
```

```cpp
#define WSEND 1
#include <hip/hip_runtime.h>
#include <utility>
#include <stdint.h>

#define NTPOL 1
#define STAG 5
#define MAP48 0
namespace {
typedef unsigned v4u __attribute__((ext_vector_type(4)));
typedef unsigned v2u __attribute__((ext_vector_type(2)));
typedef float f32x4 __attribute__((ext_vector_type(4)));
typedef float f32x2 __attribute__((ext_vector_type(2)));
typedef short bf16x8 __attribute__((ext_vector_type(8)));
typedef __bf16 bf16x2_t __attribute__((ext_vector_type(2)));
typedef __amdgpu_buffer_rsrc_t rsrc_t;
typedef unsigned short us;
#define LAS __attribute__((address_space(3)))
#define DEV __device__ __forceinline__
#define SB() __builtin_amdgcn_sched_barrier(0)
#define IC(x) std::integral_constant<int, (x)>{}
#define CV(x) decltype(x)::value

template <int B, int... I, class F> DEV void sfor_impl(std::integer_sequence<int, I...>, F&& f) { (f(std::integral_constant<int, B + I>{}), ...); }
template <int N, class F> DEV void sfor(F&& f) { sfor_impl<0>(std::make_integer_sequence<int, N>{}, f); }
template <int B, int E, class F> DEV void srange(F&& f) { sfor_impl<B>(std::make_integer_sequence<int, (E > B ? E - B : 0)>{}, f); }

constexpr int D = 1152, KS = 36;
constexpr unsigned O_EP = 0;
constexpr unsigned O_FL = 256;
constexpr int F_MU = 0, F_G = 192, F_H = 256, F_W = 384, F_KVC = 576, F_X = 704, F_T = 1024, F_V = 1280, F_P = 1536, F_S = 2560;
constexpr unsigned O_GATE = 32768;
constexpr unsigned O_MODP = O_GATE + 16384;
constexpr unsigned O_H = O_MODP + 81920;
constexpr unsigned O_WT = O_H + 64 * 72 * 1024;
constexpr unsigned O_T = O_WT + (16 * 324 + 8) * 1024;
constexpr unsigned O_VT = O_T + 32 * 144 * 1024;
constexpr unsigned O_P = O_VT + 16 * 288 * 1024;
constexpr unsigned O_ST = O_P + 128 * 32 * 1024;
constexpr unsigned O_KC = O_ST + 128 * 4096;
constexpr unsigned O_VC = O_KC + 360 * 1024;
constexpr unsigned O_WKT = O_VC + 360 * 1024;
constexpr unsigned O_VPT = O_WKT + 368 * 1024;
constexpr unsigned O_X2 = O_VPT + 432 * 1024;
constexpr unsigned O_SCP = O_X2 + 128 * 36 * 1024;
constexpr unsigned O_LNP = O_SCP + 128 * 40 * 1024;
constexpr unsigned O_U = O_LNP + 128 * 1024;
constexpr unsigned WS_BYTES = O_U + 2 * 18 * 80 * 4;
constexpr int LDS_MISC = 147456, LDS_BYTES = 163840;
constexpr int SC1 = 16;

#define STAMP(k, s)

DEV f32x4 mfma(v4u a, v4u b, f32x4 c) {
  return __builtin_amdgcn_mfma_f32_16x16x32_bf16(__builtin_bit_cast(bf16x8, a), __builtin_bit_cast(bf16x8, b), c, 0, 0, 0);
}
DEV unsigned pk2(float a, float b) { f32x2 v = {a, b}; return __builtin_bit_cast(unsigned, __builtin_convertvector(v, bf16x2_t)); }
DEV float asf(unsigned u) { return __builtin_bit_cast(float, u); }
DEV unsigned asu(float f) { return __builtin_bit_cast(unsigned, f); }
DEV float bflo(unsigned u) { return __builtin_bit_cast(float, u << 16); }
DEV float bfhi(unsigned u) { return __builtin_bit_cast(float, u & 0xffff0000u); }
DEV v4u pack8(f32x4 a, f32x4 b) { return v4u{pk2(a[0], a[1]), pk2(a[2], a[3]), pk2(b[0], b[1]), pk2(b[2], b[3])}; }
DEV v2u pack4(f32x4 a) { return v2u{pk2(a[0], a[1]), pk2(a[2], a[3])}; }
DEV rsrc_t mkrs(const void* p, unsigned bytes) { return __builtin_amdgcn_make_buffer_rsrc((void*)p, 0, bytes, 0x00020000); }
DEV void dma_m0(unsigned m, int vo, rsrc_t rs, int so) {
  asm volatile("s_mov_b32 m0, %0\n\ts_nop 0\n\tbuffer_load_dwordx4 %1, %2, %3 offen lds" ::"s"(m), "v"(vo), "s"(rs), "s"(so) : "memory");
}
DEV void dma_m0_n(unsigned m, int vo, rsrc_t rs, int so) {
  if constexpr (NTPOL) asm volatile("s_mov_b32 m0, %0\n\ts_nop 0\n\tbuffer_load_dwordx4 %1, %2, %3 offen nt lds" ::"s"(m), "v"(vo), "s"(rs), "s"(so) : "memory");
  else dma_m0(m, vo, rs, so);
}
DEV void dma_m0_h(unsigned m, int vo, rsrc_t rs, int so) {
  asm volatile("s_mov_b32 m0, %0\n\ts_nop 0\n\tbuffer_load_dwordx4 %1, %2, %3 offen sc1 lds" ::"s"(m), "v"(vo), "s"(rs), "s"(so) : "memory");
}
DEV void dma_2nd_h(int vo, rsrc_t rs, int so) {
  asm volatile("buffer_load_dwordx4 %0, %1, %2 offen offset:1024 sc1 lds" ::"v"(vo), "s"(rs), "s"(so) : "memory");
}
template <int OFF> DEV void ald16(v4u& d, int vo, rsrc_t rs, int so) {
  asm volatile("buffer_load_dwordx4 %0, %1, %2, %3 offen offset:%4 sc1" : "=v"(d) : "v"(vo), "s"(rs), "s"(so), "i"(OFF));
}
template <int OFF> DEV void ald16p(v4u& d, int vo, rsrc_t rs, int so) {
  asm volatile("buffer_load_dwordx4 %0, %1, %2, %3 offen offset:%4" : "=v"(d) : "v"(vo), "s"(rs), "s"(so), "i"(OFF));
}
template <int OFF> DEV void ald16a(v4u& d, int vo, rsrc_t rs, int so) {
  asm volatile("buffer_load_dwordx4 %0, %1, %2, %3 offen offset:%4 sc1" : "=a"(d) : "v"(vo), "s"(rs), "s"(so), "i"(OFF));
}
template <int OFF> DEV void ald8(v2u& d, int vo, rsrc_t rs, int so) {
  asm volatile("buffer_load_dwordx2 %0, %1, %2, %3 offen offset:%4 sc1" : "=v"(d) : "v"(vo), "s"(rs), "s"(so), "i"(OFF));
}
template <int OFF> DEV void dsr16(v4u& d, unsigned addr) { asm volatile("ds_read_b128 %0, %1 offset:%2" : "=v"(d) : "v"(addr), "i"(OFF)); }
template <int OFF> DEV void dsrtr(v2u& d, unsigned addr) { asm volatile("ds_read_b64_tr_b16 %0, %1 offset:%2" : "=v"(d) : "v"(addr), "i"(OFF)); }
template <int N> DEV void waitv() { asm volatile("s_waitcnt vmcnt(%0)" ::"i"(N) : "memory"); }
template <int N> DEV void waitlv() { asm volatile("s_waitcnt vmcnt(%0) lgkmcnt(0)" ::"i"(N) : "memory"); }
DEV void waitl() { asm volatile("s_waitcnt lgkmcnt(0)" ::: "memory"); }
DEV void bar() { asm volatile("s_barrier" ::: "memory"); }
DEV void lbar() { asm volatile("s_waitcnt lgkmcnt(0)\n\ts_barrier" ::: "memory"); }
DEV void pin(v4u& x) { asm volatile("" : "+v"(x)); }
DEV void pin(v2u& x) { asm volatile("" : "+v"(x)); }
DEV void pin(int& x) { asm volatile("" : "+v"(x)); }
DEV void pin(float& x) { asm volatile("" : "+v"(x)); }
DEV void pin(f32x4& x) { asm volatile("" : "+v"(x)); }
DEV void pin(f32x2& x) { asm volatile("" : "+v"(x)); }
DEV void st16(v4u v, rsrc_t rs, int vo, int so) { asm volatile("buffer_store_dwordx4 %0, %1, %2, %3 offen sc1\n\ts_nop 1" ::"v"(v), "v"(vo), "s"(rs), "s"(so) : "memory"); }
template <int OFF> DEV void st16o(v4u v, rsrc_t rs, int vo) { asm volatile("buffer_store_dwordx4 %0, %1, %2, 0 offen offset:%3\n\ts_nop 1" ::"v"(v), "v"(vo), "s"(rs), "i"(OFF) : "memory"); }
DEV float max3(float a, float b, float c) { float r; asm("v_max3_f32 %0, %1, %2, %3" : "=v"(r) : "v"(a), "v"(b), "v"(c)); return r; }
DEV float max4(f32x4 a) { return max3(max3(a[0], a[1], a[2]), a[3], a[3]); }
DEV void st8(v2u v, rsrc_t rs, int vo, int so) { __builtin_amdgcn_raw_buffer_store_b64(v, rs, vo, so, SC1); }
DEV v4u ld16(rsrc_t rs, int vo, int so) { return __builtin_amdgcn_raw_buffer_load_b128(rs, vo, so, 0); }
DEV v4u ld16h(rsrc_t rs, int vo, int so) { return __builtin_amdgcn_raw_buffer_load_b128(rs, vo, so, SC1); }
struct FMax { DEV float operator()(float a, float b) const { return max3(a, b, b); } };
struct FAdd { DEV float operator()(float a, float b) const { return a + b; } };
template <class Op> DEV float sw32(float a, float b, Op op) { asm("s_nop 1\n\tv_permlane32_swap_b32 %0, %1" : "+v"(a), "+v"(b)); return op(a, b); }
template <class Op> DEV float sw16(float a, float b, Op op) { asm("s_nop 1\n\tv_permlane16_swap_b32 %0, %1" : "+v"(a), "+v"(b)); return op(a, b); }
DEV float xmax16_32(float v) { v = sw16(v, v, FMax{}); return sw32(v, v, FMax{}); }
DEV float xsum16_32(float v) { v = sw16(v, v, FAdd{}); return sw32(v, v, FAdd{}); }
template <class Op> DEV float xred4(float v0, float v1, float v2, float v3, Op op) { return sw16(sw32(v0, v2, op), sw32(v1, v3, op), op); }
DEV void touch(unsigned lb, int vo, rsrc_t rs, int so) {
  asm volatile("s_mov_b32 m0, %0\n\ts_nop 0\n\tbuffer_load_dword %1, %2, %3 offen lds" ::"s"(lb + LDS_BYTES - 256), "v"(vo), "s"(rs), "s"(so) : "memory");
}
template <bool HA, bool H_IS_A, bool FIRST> DEV void mma_h(f32x4& acc, const v4u& h, const v4u& o) {
#define MMA_(CH, X, Y) \
  if constexpr (FIRST) asm volatile("v_mfma_f32_16x16x32_bf16 %0, " X ", " Y ", 0" : "=&v"(acc) : CH(h), "v"(o)); \
  else asm volatile("v_mfma_f32_16x16x32_bf16 %0, " X ", " Y ", %0" : "+v"(acc) : CH(h), "v"(o));
  if constexpr (HA) { if constexpr (H_IS_A) { MMA_("a", "%1", "%2") } else { MMA_("a", "%2", "%1") } }
  else { if constexpr (H_IS_A) { MMA_("v", "%1", "%2") } else { MMA_("v", "%2", "%1") } }
#undef MMA_
}
DEV void mfma_settle() { asm volatile("s_nop 15" ::: "memory"); SB(); }

struct Ctx {
  char* ws; rsrc_t rs; unsigned lb; LAS char* lds; int lane, w; unsigned t0, t1;
  DEV void make_tag() {
    const v2u ep = *(volatile LAS v2u*)(lds + LDS_BYTES - 512 - w * 256);
    const unsigned long long t = (((unsigned long long)(unsigned)__builtin_amdgcn_readfirstlane(ep[1]) << 32) | (unsigned)__builtin_amdgcn_readfirstlane(ep[0])) + 0x9E3779B97F4A7C15ull;
    t0 = (unsigned)t; t1 = (unsigned)(t >> 32);
  }
  DEV v2u tag() const { return v2u{t0, t1}; }
};
DEV void flag_set(const Ctx& c, int f) { if (c.lane == 0) __builtin_amdgcn_raw_buffer_store_b64(c.tag(), c.rs, 0, O_FL + f * 8, SC1); }
DEV bool flag_ok(const Ctx& c, v2u v) { return __all(v[0] == c.t0 && v[1] == c.t1); }
DEV void flag_spin(const Ctx& c, int f) {
#pragma unroll 1
  for (int i = 0; i < 20000; ++i) {
    v2u v;
    asm volatile("buffer_load_dwordx2 %0, %1, %2, 0 offen offset:%3 sc1\n\ts_waitcnt vmcnt(0)" : "=&v"(v) : "v"(f * 8), "s"(c.rs), "i"(O_FL) : "memory");
    if (flag_ok(c, v)) return;
    __builtin_amdgcn_s_sleep(1);
  }
}
DEV void publish(const Ctx& c, int f) { waitv<0>(); bar(); if (c.w == 0) flag_set(c, f); }
DEV void publish_first(Ctx& c, int f) { waitv<0>(); c.make_tag(); bar(); if (c.w == 0) flag_set(c, f); }

constexpr int lo_of(int m, int n, int span) { int r = 0; while (r < n && (r * span) / n < m) ++r; return r; }
constexpr int mid_of(int m, int n, int nm) { int q = 0; while (q < n && ((2 * q + 1) * nm) / (2 * n) < m) ++q; return q; }
template <int ROW, int DS, int NTP, int NK, int NP, int NR, int NM, class Hook, class Issue, class Rd, class Pin, class Mm>
DEV void raw_loop(const Ctx& c, Hook&& hook, Issue&& issue, Rd&& rd, Pin&& pinf, Mm&& mm) {
  constexpr int NS = KS / NK;
  STAMP(ROW, 1);
  sfor<2>([&](auto sc) { sfor<NP>([&](auto pc) { issue(sc, pc); }); });
  hook(IC(-1));
  if constexpr (DS == 3) sfor<NP>([&](auto pc) { issue(IC(2), pc); });
  SB();
  STAMP(ROW, 2);
  waitv<(DS - 1) * NP + NTP>(); bar();
  STAMP(ROW, 3);
  sfor<NR>([&](auto rc) { rd(IC(0), rc); });
  SB();
  sfor<KS>([&](auto kc) {
    constexpr int k = CV(kc), s = k / NK, kk = k % NK;
    constexpr int p0 = (kk * NP) / NK, p1 = ((kk + 1) * NP) / NK, np = (s + DS < NS) ? p1 - p0 : 0;
    if constexpr (kk == NK - 1 && s + 1 < NS) { waitlv<(s + DS < NS ? p0 : 0) + (DS == 3 && s + 2 < NS ? NP : 0) + (s == 0 ? NTP : 0)>(); bar(); }
    else waitl();
    pinf(kc);
    hook(kc);
    SB();
    sfor<NM>([&](auto mc) {
      constexpr int m = CV(mc), span = NM - NM / 4;
      mm(kc, mc);
      SB();
      if constexpr (k + 1 < KS) srange<lo_of(m, NR, span), lo_of(m + 1, NR, span)>([&](auto rc) { rd(IC(k + 1), rc); SB(); });
      srange<mid_of(m, np, NM), mid_of(m + 1, np, NM)>([&](auto qc) { issue(IC(s + DS), IC(p0 + CV(qc))); SB(); });
    });
    if constexpr (k == 11) STAMP(ROW, 4);
    if constexpr (k == 23) STAMP(ROW, 5);
  });
  STAMP(ROW, 6);
}

DEV unsigned kc_rd(unsigned base, int lane, int h) { const int r = lane & 15, q = lane >> 4; return base + r * 128 + (((4 * h + q) ^ (r & 7)) << 4); }
template <bool PI> DEV int kc_src(int lane, int stride) { const int rr = lane >> 3; return (PI ? 8 * (rr >> 2) + (rr & 3) : rr) * stride + (((lane & 7) ^ rr) << 4); }
DEV int tr64_src(int lane, int w, int stride) { const int rr = lane >> 3; return rr * stride + (((lane & 7) ^ ((((rr >> 1) & 1) | ((w & 1) << 1)) << 1)) << 4); }
DEV unsigned tr64_rd(unsigned base, int lane, int w) {
  const int gg = lane >> 4, qq = (lane & 15) >> 2, pp = lane & 3;
  return base + (8 * gg + qq) * 128 + (((2 * w + (pp >> 1)) ^ (((qq >> 1) | ((gg & 1) << 1)) << 1)) << 4) + 8 * (pp & 1);
}
DEV int pi_inv_off(int t, int lane, int blk0, int blk1) {
  const int xx = 16 * (t & 1) + (lane & 15);
  return (((xx >> 2) & 1) ? blk1 : blk0) + (((lane >> 4) * 16 + 4 * (xx >> 3) + (xx & 3)) << 4);
}
DEV v4u cat(v2u a, v2u b) { return v4u{a[0], a[1], b[0], b[1]}; }
DEV v4u silu8(v4u v) {
  auto f = [](float x) { return x * __builtin_amdgcn_rcpf(1.f + __builtin_amdgcn_exp2f(x * -1.4426950408889634f)); };
  v4u o;
  for (int e = 0; e < 4; ++e) o[e] = pk2(f(bflo(v[e])), f(bfhi(v[e])));
  return o;
}

template <int TRB> DEV void w_tile(Ctx& c, const us* wqkv, const us* wo, const us* cin, const us* wmod, int ti, int tj, int pi, int pj, int mu) {
  const int lane = c.lane, w = c.w, wr = w >> 1, wc = w & 1;
  const rsrc_t ra = mkrs(wqkv, D * 3456 * 2), rb = TRB ? mkrs(wo, D * D * 2) : ra;
  const unsigned lb = c.lb;
  const int va = kc_src<true>(lane, 6912);
  const int sa = ti * 128 * 6912 + (TRB ? 4608 : 0) + (16 * (w & 1) + 4 * (w >> 1)) * 6912;
  int vb, sb;
  if constexpr (TRB) { vb = (lane >> 4) * 2304 + (((lane & 15) ^ (((lane >> 4) << 2) | w)) << 4); sb = tj * 256 + 4 * w * 2304; }
  else { vb = kc_src<false>(lane, 6912); sb = (tj * 128 + 8 * w) * 6912 + 2304; }
  unsigned ra_[2][2], rb_[2][8];
  sfor<2>([&](auto gc) { constexpr int g = CV(gc);
    sfor<2>([&](auto hc) { ra_[g][CV(hc)] = kc_rd(lb + g * 65536 + wr * 8192, lane, CV(hc)); });
    if constexpr (TRB) {
      const int gg = lane >> 4, qq = (lane & 15) >> 2, pp = lane & 3;
      sfor<8>([&](auto ec) { constexpr int cc = CV(ec) >> 1, t = CV(ec) & 1;
        const int row = 8 * gg + 4 * t + qq, ch = 2 * (4 * wc + cc) + (pp >> 1);
        rb_[g][CV(ec)] = lb + g * 65536 + 16384 + 256 * row + 16 * (ch ^ ((qq << 2) | ((2 * gg + t) & 3))) + 8 * (pp & 1); });
    } else sfor<2>([&](auto hc) { rb_[g][CV(hc)] = kc_rd(lb + g * 65536 + 16384 + wc * 8192, lane, CV(hc)); }); });
  const int so0 = STAG == 5 ? ((pi + pj) * 18 / 5) % 18 : STAG == 2 ? ((pi + pj) & 1) * 9 : 0;
  auto stg = [&](int st) { const int t = st + so0; return t >= 18 ? t - 18 : t; };
  int eo[4][2];
  const int ks = ti * 4 + wr * 2;
  auto blk = [&](int T, int v) {
    if constexpr (TRB) return T < 60 ? (T / 5) * 324 + 144 + (ks + v) * 5 + T % 5 : (12 + (T - 60) / 3) * 324 + 216 + (ks + v) * 3 + (T - 60) % 3;
    else return T < 48 ? (T >> 2) * 324 + (ks + v) * 4 + (T & 3) : (12 + (T - 48) / 6) * 324 + (ks + v) * 6 + (T - 48) % 6; };
  const int strip = mu >> 2, kq = mu & 3, dead = mu < 144 ? 0 : 0x7ff00000;
  v4u cv;
  ald16p<0>(cv, (lane < 36 && w < 2 ? lane * 16 : 0x7ff00000) | dead, mkrs(cin, 2 * D * 2), (w & 1) * 2304 + kq * 576);
  { const rsrc_t rw = mkrs(wmod, D * 6912 * 2);
    const int v64 = tr64_src(lane, w, 13824) | dead, s64 = strip * 128 + (kq * 288 + 8 * w) * 13824;
    sfor<9>([&](auto pc) { dma_m0_n(lb + 98304 + (w + 4 * CV(pc)) * 1024, v64, rw, s64 + CV(pc) * 32 * 13824); }); }
  v4u fa[2][4], fb[2][4];
  v2u ft[2][8];
  f32x4 acc[4][4];
  raw_loop<0, 3, 1, 2, 8, TRB ? 12 : 8, 16>(c,
    [&](auto kc) {
      if constexpr (CV(kc) == -1) {
        waitv<25>();
        pin(cv);
        LAS v4u* sc = (LAS v4u*)(c.lds + LDS_MISC);
        if (w < 2) { if (lane < 36) sc[w * 36 + lane] = silu8(cv); } else if (lane == 0) sc[72] = v4u{0, 0, 0, 0};
        waitlv<16>(); bar();
        const int r = lane & 15, q = lane >> 4;
        const unsigned cb = lb + LDS_MISC + (r < 2 ? r * 576 + q * 16 : 1152), cs = r < 2 ? 64 : 0, ra2 = tr64_rd(lb + 98304, lane, w);
        v2u mt[9][2]; v4u cf[9];
        sfor<9>([&](auto ic) { constexpr int i = CV(ic); dsrtr<i * 4096>(mt[i][0], ra2); dsrtr<i * 4096 + 512>(mt[i][1], ra2); dsr16<0>(cf[i], cb + i * cs); });
        waitl();
        f32x4 m = {0, 0, 0, 0};
        sfor<9>([&](auto ic) { constexpr int i = CV(ic); pin(mt[i][0]); pin(mt[i][1]); pin(cf[i]); m = mfma(cat(mt[i][0], mt[i][1]), cf[i], m); });
        pin(m); mfma_settle();
        st16(__builtin_bit_cast(v4u, m), c.rs, (r < 2 ? r * 36864 + q * 16 : 0x7ff00000) | dead, O_MODP + kq * 9216 + strip * 256 + w * 64);
      }
      if constexpr (CV(kc) == 3) { c.make_tag(); if (w == 0 && mu < 144) flag_set(c, F_MU + mu); }
      if constexpr (CV(kc) == -1) sfor<4>([&](auto jc) { constexpr int j = CV(jc); const int T0 = tj * 8 + wc * 4 + (j & 2);
      sfor<2>([&](auto vc) { constexpr int v = CV(vc); eo[j][v] = pi_inv_off(j, lane, blk(T0, v) * 1024, blk(T0 + 1, v) * 1024); pin(eo[j][v]); }); }); },
    [&](auto sc, auto pc) { constexpr int s = CV(sc), p = CV(pc), u = p >> 1;
      const unsigned m = lb + (s & 3) * 32768 + (p & 1) * 16384 + u * 4096 + w * 1024;
      if constexpr ((p & 1) == 0) dma_m0(m, va, ra, sa + u * 32 * 6912 + stg(s) * 128);
      else if constexpr (TRB) dma_m0(m, vb, rb, sb + u * 16 * 2304 + stg(s) * (64 * 2304));
      else dma_m0(m, vb, rb, sb + u * 32 * 6912 + stg(s) * 128); },
    [&](auto kc, auto rc) { constexpr int k = CV(kc), r = CV(rc), s = (k >> 1) & 3, h = k & 1;
      if constexpr (r < 4) dsr16<(s & 1) * 32768 + r * 2048>(fa[k & 1][r], ra_[s >> 1][h]);
      else if constexpr (TRB) dsrtr<(s & 1) * 32768 + h * 8192>(ft[k & 1][r - 4], rb_[s >> 1][r - 4]);
      else dsr16<(s & 1) * 32768 + (r - 4) * 2048>(fb[k & 1][r - 4], rb_[s >> 1][h]); },
    [&](auto kc) { constexpr int k = CV(kc);
      sfor<4>([&](auto ic) { pin(fa[k & 1][CV(ic)]); });
      if constexpr (TRB) sfor<4>([&](auto ic) { constexpr int i = CV(ic); pin(ft[k & 1][2 * i]); pin(ft[k & 1][2 * i + 1]); fb[k & 1][i] = cat(ft[k & 1][2 * i], ft[k & 1][2 * i + 1]); });
      else sfor<4>([&](auto ic) { pin(fb[k & 1][CV(ic)]); }); },
    [&](auto kc, auto mc) { constexpr int k = CV(kc), i = CV(mc) & 3, j = CV(mc) >> 2;
      acc[i][j] = mfma(fa[k & 1][i], fb[k & 1][j], k == 0 ? f32x4{0, 0, 0, 0} : acc[i][j]); });
  sfor<4>([&](auto jc) { constexpr int j = CV(jc);
    sfor<2>([&](auto vc) { constexpr int v = CV(vc); st16(pack8(acc[2 * v][j], acc[2 * v + 1][j]), c.rs, eo[j][v], O_WT); }); });
  STAMP(0, 7);
  publish(c, F_W + (TRB * 9 + tj) * 9 + ti);
  STAMP(0, 8);
}

template <int KIND> DEV void small_tile(Ctx& c, const us* wgt, const us* y, int b, int t, int rot) {
  constexpr bool TR = KIND != 0;
  constexpr int LD = KIND == 2 ? 4608 : D * 2;
  const int lane = c.lane, w = c.w;
  const rsrc_t r64 = mkrs(wgt, D * LD), r80 = KIND == 2 ? mkrs(y, 154 * D * 2) : c.rs;
  const int v64 = TR ? tr64_src(lane, w, LD) : kc_src<true>(lane, LD);
  const int s64 = TR ? t * 128 + 8 * w * LD : (t * 64 + 16 * (w & 1) + 4 * (w >> 1)) * LD;
  const int v80 = KIND == 2 ? kc_src<false>(lane, D * 2) : lane * 16, v80x = (KIND != 2 || (lane >> 3) < 5 || !(w & 1)) ? v80 : 0x7ff00000;
  const int s80 = KIND == 2 ? (b * 77 + (w & 1) * 40) * (D * 2) + (w >> 1) * 128 : (KIND ? O_VC : O_KC) + b * (180 * 1024) + w * 5120;
  auto stg = [&](int st) { const int u = st + rot; return u >= 9 ? u - 9 : u; };
  unsigned r64a[3][2], r80a[3][2];
  sfor<3>([&](auto gc) { constexpr int g = CV(gc); const unsigned base = c.lb + g * 49152;
    if constexpr (TR) r64a[g][0] = tr64_rd(base, lane, w); else sfor<2>([&](auto hc) { r64a[g][CV(hc)] = kc_rd(base + w * 2048, lane, CV(hc)); });
    if constexpr (KIND == 2) sfor<2>([&](auto hc) { r80a[g][CV(hc)] = kc_rd(base, lane, CV(hc)); }); else r80a[g][0] = base + lane * 16; });
  v4u f64[2], f80[2][5];
  v2u ft[2][2];
  f32x4 acc[5];
  if constexpr (KIND != 2) flag_spin(c, F_KVC + (KIND * 2 + b) * 18 + (lane < 18 ? lane : 0));
  raw_loop<KIND == 2 ? 2 : 4, 2, 0, 4, 9, TR ? 7 : 6, 5>(c, [](auto) {},
    [&](auto sc, auto pc) { constexpr int s = CV(sc), p = CV(pc);
      const unsigned m = c.lb + (s & 3) * 36864;
      if constexpr (p < 4) {
        if constexpr (TR) dma_m0(m + (w + 4 * p) * 1024, v64, r64, s64 + 32 * p * LD + stg(s) * (128 * LD));
        else dma_m0(m + (p >> 1) * 8192 + (w + 4 * (p & 1)) * 1024, v64, r64, s64 + 32 * (p & 1) * LD + (p >> 1) * 128 + stg(s) * 256);
      } else if constexpr (KIND == 2) dma_m0(m + 16384 + (5 * w + p - 4) * 1024, p == 8 ? v80x : v80, r80, s80 + (p - 4) * 8 * (D * 2) + stg(s) * 256);
      else dma_m0_h(m + 16384 + (5 * w + p - 4) * 1024, v80, r80, s80 + (p - 4) * 1024 + stg(s) * 20480); },
    [&](auto kc, auto rc) { constexpr int k = CV(kc), r = CV(rc), kk = k & 3, so = ((k >> 2) & 3) * 36864;
      if constexpr (r < 5) { constexpr int o = so + 16384 + (KIND == 2 ? (kk >> 1) * 10240 + r * 2048 : (kk * 5 + r) * 1024); dsr16<o % 49152>(f80[k & 1][r], r80a[o / 49152][KIND == 2 ? kk & 1 : 0]); }
      else if constexpr (TR) { constexpr int o = so + kk * 4096 + (r - 5) * 512; dsrtr<o % 49152>(ft[k & 1][r - 5], r64a[o / 49152][0]); }
      else { constexpr int o = so + (kk >> 1) * 8192; dsr16<o % 49152>(f64[k & 1], r64a[o / 49152][kk & 1]); } },
    [&](auto kc) { constexpr int k = CV(kc);
      sfor<5>([&](auto ic) { pin(f80[k & 1][CV(ic)]); });
      if constexpr (TR) { pin(ft[k & 1][0]); pin(ft[k & 1][1]); f64[k & 1] = cat(ft[k & 1][0], ft[k & 1][1]); } else pin(f64[k & 1]); },
    [&](auto kc, auto mc) { constexpr int k = CV(kc), m = CV(mc);
      const f32x4 z = k == 0 ? f32x4{0, 0, 0, 0} : acc[m];
      acc[m] = KIND == 1 ? mfma(f80[k & 1][m], f64[k & 1], z) : mfma(f64[k & 1], f80[k & 1][m], z); });
  if constexpr (KIND == 2) {
    const int isv = t >= 18, n0 = (t - 18 * isv) * 64 + 16 * w, q = lane >> 4;
    const int vo = ((2 * (w & 1) + (q >> 1)) * 16 + (lane & 15)) * 16 + (q & 1) * 8;
    sfor<5>([&](auto tc) { st8(pack4(acc[CV(tc)]), c.rs, vo, (isv ? O_VC : O_KC) + ((b * 36 + (n0 >> 5)) * 5 + CV(tc)) * 1024); });
    publish_first(c, F_KVC + (isv * 2 + b) * 18 + t - 18 * isv);
  } else if constexpr (KIND == 1) {
    const int T0 = b * 72 + t * 4 + (w & 2);
    const int off = pi_inv_off(w, lane, T0 * 3072, (T0 + 1) * 3072);
    st16(pack8(acc[0], acc[1]), c.rs, off, O_VPT);
    st16(pack8(acc[2], acc[3]), c.rs, off, O_VPT + 1024);
    st16(pack8(acc[4], f32x4{0, 0, 0, 0}), c.rs, off, O_VPT + 2048);
    publish(c, F_X + (2 + b) * 18 + t);
  } else {
    LAS float* red = (LAS float*)(c.lds + LDS_MISC);
    sfor<5>([&](auto tc) { v2u v = pack4(acc[CV(tc)]);
      if constexpr (CV(tc) == 4) { if ((lane & 15) == 13) v = v2u{0x3f803f80u, 0x3f803f80u}; }
      st8(v, c.rs, lane * 16 + (w & 1) * 8, O_WKT + ((b * 36 + 2 * t + (w >> 1)) * 5 + CV(tc)) * 1024);
      const float s = xsum16_32((bflo(v[0]) + bfhi(v[0])) + (bflo(v[1]) + bfhi(v[1])));
      if (lane < 16) red[w * 80 + CV(tc) * 16 + lane] = s; });
    lbar();
    const int tid = w * 64 + lane;
    if (tid < 80) __builtin_amdgcn_raw_buffer_store_b32(asu((red[tid] + red[80 + tid]) + (red[160 + tid] + red[240 + tid])), c.rs, tid * 4, O_U + (b * 18 + t) * 320, SC1);
    publish(c, F_X + b * 18 + t);
  }
}

DEV void gate_strip(Ctx& c, const us* cin, const us* wmod, int strip) {
  const int lane = c.lane, w = c.w, r = lane & 15, q = lane >> 4;
  const rsrc_t rw = mkrs(wmod, D * 6912 * 2), rc = mkrs(cin, 2 * D * 2);
  v4u cv, cv2;
  ald16p<0>(cv, lane * 16, rc, w * 1152);
  ald16p<1024>(cv2, lane < 8 ? lane * 16 : 0x7ff00000, rc, w * 1152);
  const int v64 = tr64_src(lane, w, 13824), s64 = 4608 + strip * 128 + 8 * w * 13824;
  sfor<36>([&](auto pc) { dma_m0_n(c.lb + (w + 4 * CV(pc)) * 1024, v64, rw, s64 + CV(pc) * 32 * 13824); });
  waitv<36>();
  pin(cv); pin(cv2);
  LAS v4u* sc = (LAS v4u*)(c.lds + LDS_MISC);
  sc[w * 72 + lane] = silu8(cv);
  if (lane < 8) sc[w * 72 + 64 + lane] = silu8(cv2);
  if (lane == 0) sc[288] = v4u{0, 0, 0, 0};
  const unsigned cb = c.lb + LDS_MISC + (r < 2 ? r * 2304 + q * 16 : 4608), cs = r < 2 ? 64 : 0;
  f32x4 acc = {0, 0, 0, 0};
  sfor<3>([&](auto gc) { constexpr int g = CV(gc);
    waitlv<24 - 12 * g>(); bar();
    const unsigned ra = tr64_rd(c.lb + g * 49152, lane, w);
    v2u ft[12][2]; v4u cf[12];
    sfor<12>([&](auto kc) { constexpr int k = CV(kc); dsrtr<k * 4096>(ft[k][0], ra); dsrtr<k * 4096 + 512>(ft[k][1], ra); dsr16<0>(cf[k], cb + (12 * g + k) * cs); });
    waitl();
    sfor<12>([&](auto kc) { constexpr int k = CV(kc); pin(ft[k][0]); pin(ft[k][1]); pin(cf[k]); acc = mfma(cat(ft[k][0], ft[k][1]), cf[k], acc); }); });
  const v2u pv = pack4(acc);
  if (r < 2) st16(v4u{pv[0] << 16, pv[0] & 0xffff0000u, pv[1] << 16, pv[1] & 0xffff0000u}, c.rs, r * 4608 + q * 16, O_GATE + strip * 256 + w * 64);
  publish(c, F_G + strip);
}

template <int N> DEV void ln_load(const Ctx& c, const us* xin, int rt0, v4u (&xv)[N][9]) {
  const rsrc_t rx = mkrs(xin, 2048 * D * 2);
  sfor<9 * N>([&](auto ic) { constexpr int n = CV(ic) / 9, k = CV(ic) % 9;
    ald16p<k * 64>(xv[n][k], (c.lane & 15) * (D * 2) + (c.lane >> 4) * 16, rx, (rt0 + n) * (16 * D * 2) + 9 * c.w * 64); });
}
template <int N> DEV void ln_tiles(const Ctx& c, int rt0, v4u (&xv)[N][9]) {
  const int lane = c.lane, w = c.w, r = lane & 15, q = lane >> 4;
  STAMP(3, 1);
  LAS f32x2* red = (LAS f32x2*)(c.lds + LDS_MISC + 8192);
  sfor<N>([&](auto nc) { constexpr int n = CV(nc);
    f32x4 s1 = {0, 0, 0, 0}, s2 = {0, 0, 0, 0};
    sfor<9>([&](auto kc) { constexpr int k = CV(kc); pin(xv[n][k]);
      s1 = mfma(v4u{0x3f803f80u, 0x3f803f80u, 0x3f803f80u, 0x3f803f80u}, xv[n][k], s1); s2 = mfma(xv[n][k], xv[n][k], s2); });
    const int e = r & 3;
    if (q == (r >> 2)) red[(n * 4 + w) * 16 + r] = f32x2{s1[0], e == 0 ? s2[0] : e == 1 ? s2[1] : e == 2 ? s2[2] : s2[3]}; });
  lbar();
  f32x2 nx[N][9][4];
  sfor<N>([&](auto nc) { constexpr int n = CV(nc);
    const f32x2 s = (red[n * 64 + r] + red[n * 64 + 16 + r]) + (red[n * 64 + 32 + r] + red[n * 64 + 48 + r]);
    const float mean = s[0] * (1.f / D), rs = __builtin_amdgcn_rsqf(fmaxf(s[1] * (1.f / D) - mean * mean, 0.f) + 1e-5f), nm = -mean * rs;
    sfor<9>([&](auto kc) { constexpr int k = CV(kc); for (int e = 0; e < 4; ++e) { nx[n][k][e] = f32x2{bflo(xv[n][k][e]), bfhi(xv[n][k][e])} * rs + nm; pin(nx[n][k][e]); } }); });
  STAMP(3, 2);
#pragma unroll 1
  for (int i = 0; i < 20000; ++i) {
    v2u v0, v1, v2;
    asm volatile("buffer_load_dwordx2 %0, %3, %4, 0 offen offset:%5 sc1\n\tbuffer_load_dwordx2 %1, %3, %4, 0 offen offset:%6 sc1\n\t"
                 "buffer_load_dwordx2 %2, %3, %4, 0 offen offset:%7 sc1\n\ts_waitcnt vmcnt(0)"
                 : "=&v"(v0), "=&v"(v1), "=&v"(v2) : "v"((lane < 48 ? lane : 0) * 8), "s"(c.rs), "i"(O_FL + F_MU * 8), "i"(O_FL + F_MU * 8 + 384), "i"(O_FL + F_MU * 8 + 768) : "memory");
    if (flag_ok(c, v0) && flag_ok(c, v1) && flag_ok(c, v2)) break;
    __builtin_amdgcn_s_sleep(1);
  }
  STAMP(3, 3);
  { v4u pv[3][4];
    const int mb = O_MODP + (rt0 >> 6) * 36864 + w * 1152;
    sfor<4>([&](auto uc) { constexpr int u = CV(uc);
      ald16<0>(pv[0][u], lane * 16, c.rs, mb + u * 9216); ald16<0>(pv[1][u], lane * 16, c.rs, mb + u * 9216 + 4608);
      ald16<1024>(pv[2][u], lane < 16 ? (lane & 7) * 16 + (lane >> 3) * 4608 : 0x7ff00000, c.rs, mb + u * 9216); });
    waitv<0>();
    LAS f32x4* ms = (LAS f32x4*)(c.lds + w * 2304);
    sfor<3>([&](auto ic) { constexpr int i = CV(ic);
      sfor<4>([&](auto uc) { pin(pv[i][CV(uc)]); });
      const v2u p = pack4((__builtin_bit_cast(f32x4, pv[i][0]) + __builtin_bit_cast(f32x4, pv[i][1])) + (__builtin_bit_cast(f32x4, pv[i][2]) + __builtin_bit_cast(f32x4, pv[i][3])));
      const f32x4 v = f32x4{bflo(p[0]), bfhi(p[0]), bflo(p[1]), bfhi(p[1])} + (i == 0 ? 0.f : i == 1 ? 1.f : (float)((lane >> 3) & 1));
      if constexpr (i < 2) ms[i * 72 + lane] = v; else if (lane < 16) ms[(lane >> 3) * 72 + 64 + (lane & 7)] = v; });
    waitl();
  }
  const unsigned ma = c.lb + w * 2304 + q * 32;
  v4u mv[2][4];
  sfor<4>([&](auto ic) { dsr16<(CV(ic) >> 1) * 1152 + (CV(ic) & 1) * 16>(mv[0][CV(ic)], ma); });
  sfor<9>([&](auto kc) { constexpr int k = CV(kc);
    waitl();
    sfor<4>([&](auto ic) { pin(mv[k & 1][CV(ic)]); });
    if constexpr (k + 1 < 9) sfor<4>([&](auto ic) { dsr16<(CV(ic) >> 1) * 1152 + (CV(ic) & 1) * 16 + (k + 1) * 128>(mv[(k + 1) & 1][CV(ic)], ma); });
    sfor<N>([&](auto nc) { constexpr int n = CV(nc);
      v4u o;
      sfor<4>([&](auto ec) { constexpr int e = CV(ec); const v4u t = mv[k & 1][e >> 1], a = mv[k & 1][2 + (e >> 1)];
        const f32x2 v = nx[n][k][e] * f32x2{asf(a[2 * (e & 1)]), asf(a[2 * (e & 1) + 1])} + f32x2{asf(t[2 * (e & 1)]), asf(t[2 * (e & 1) + 1])}; o[e] = pk2(v[0], v[1]); });
      st16(o, c.rs, lane * 16, O_H + ((((rt0 + n) >> 1) * 36 + 9 * w + k) * 2 + ((rt0 + n) & 1)) * 1024); });
    SB(); });
  STAMP(3, 4);
  waitv<0>(); bar();
  STAMP(3, 5);
  if (w == 0 && lane < N) __builtin_amdgcn_raw_buffer_store_b64(c.tag(), c.rs, lane * 8, O_FL + (F_H + rt0) * 8, SC1);
}

enum { OP_OCT, OP_A, OP_POLL, OP_ST };
struct Plan {
  static constexpr int MAXO = 200, MAXK = 110;
  int n = 0, kind[MAXO] = {}, idx[MAXO] = {}, wt[MAXO] = {}, need[MAXO] = {};
  int in0[MAXK] = {}, in1[MAXK] = {}, cnt[MAXK] = {}, pre = 0, cnt_pre = 0;
  bool ok = true;
  constexpr void add(int k, int i, int w, int nd) { kind[n] = k; idx[n] = i; wt[n] = w; need[n] = nd; ++n; }
  constexpr int left(int upto, int t) const {
    int last = -1, s = 0;
    for (int i = 0; i < upto; ++i) if (need[i] <= t) last = i;
    for (int i = last + 1; i < upto; ++i) s += wt[i];
    return s;
  }
};
template <int NT> struct P1 {
  static constexpr int NV = 9 - NT, NK = 108, NOCT = 59;
  static constexpr int G_W2 = 8, G_T1 = NT == 4 ? 65 : 61, G_T2 = 80, G_V = 108, FL_T = NT == 4 ? 44 : 48, FL_V = 80;
  static constexpr int nb(int k) { return k < 36 ? NT : k < 72 ? NV : 4; }
  static constexpr int piece(int k, int j) { return k < 36 ? k * NT + j : k < 72 ? 36 * NT + (k - 36) * NV + j : 328 + (k - 72) * 4 + j; }
  static constexpr int kof(int p) { return p < 36 * NT ? p / NT : p < 324 ? 36 + (p - 36 * NT) / NV : p < 328 ? 71 : 72 + (p - 328) / 4; }
  static constexpr bool isbar(int k) { return k % 4 == 0 || k < 4 || (k >= 70 && k <= 75); }
  static constexpr Plan make() {
    Plan p;
    auto need = [](int o) { int k = kof(8 * o) - 1; while (k >= 0 && !isbar(k)) --k; return k; };
    auto gate = [](int o) { return o >= 53 ? G_T2 : o >= 41 ? G_T1 : 8 * o + 7 >= 36 * NT ? G_W2 : 0; };
    int o = 0;
    for (; o < 3; ++o) p.add(OP_OCT, o, 2, need(o));
    p.pre = p.n; p.cnt_pre = p.left(p.n, -1);
    for (int k = 0; k < NK; ++k) {
      if (k == 36) p.add(OP_ST, 0, NT, FL_T);
      if (k == 72) p.add(OP_ST, 1, NV, FL_V);
      p.cnt[k] = p.left(p.n, k);
      p.in0[k] = p.n;
      if (k == G_W2 - 4) p.add(OP_POLL, 0, 1, G_W2);
      if (k == G_T1 - 5) p.add(OP_POLL, 1, 1, G_T1);
      if (k == G_T2 - 5) p.add(OP_POLL, 2, 1, G_T2);
      if (k == G_V - 6) p.add(OP_POLL, 3, 1, G_V);
      if (o < NOCT && gate(o) <= k) {
        bool free_ = o < 18;
        if (!free_) for (int kb = kof(8 * (o - 18) + 7); kb <= k; ++kb) if (isbar(kb)) free_ = true;
        if (free_) { if (need(o) <= k) p.ok = false; p.add(OP_OCT, o, 2, need(o)); ++o; }
      }
      p.in1[k] = p.n;
      if (p.cnt[k] > 60) p.ok = false;
    }
    if (o != NOCT) p.ok = false;
    return p;
  }
};
struct P3 {
  static constexpr int NK = 32, DIST = 5, AW = 7;
  static constexpr bool isbar(int k) { return k % 2 == 1; }
  static constexpr Plan make() {
    Plan p;
    for (int k = 0; k < DIST; ++k) p.add(OP_A, k, 1, k);
    p.pre = p.n;
    int o = 18;
    for (int k = 0; k < NK; ++k) {
      p.cnt[k] = p.left(p.n, k);
      p.in0[k] = p.n;
      if (k + DIST < NK) p.add(OP_A, k + DIST, 1, k + DIST);
      if (o < 40) {
        bool free_ = false;
        for (int kb = (8 * (o - 18) + 7) / 9; kb <= k; ++kb) if (isbar(kb)) free_ = true;
        int nd = o < 36 ? (8 * o) / 9 - 1 : NK;
        while (nd < NK && !isbar(nd)) --nd;
        if (free_) { if (nd <= k) p.ok = false; p.add(OP_OCT, o, 2, nd); ++o; }
      }
      p.in1[k] = p.n;
    }
    if (o != 40) p.ok = false;
    return p;
  }
};

template <int PAR> DEV void k3_epi(const Ctx& c, f32x4 (&o)[9], f32x4 (&xf)[10], v4u (&gv)[10], int nb, int qt) {
  const int lane = c.lane, vo = lane * 16, ks0 = (9 * nb) >> 1;
  v4u wk[5][5];
  sfor<25>([&](auto ic) { dsr16<CV(ic) * 1024>(wk[CV(ic) / 5][CV(ic) % 5], c.lb + vo); });
  v4u x2[5];
  sfor<5>([&](auto uc) { constexpr int u = CV(uc);
    constexpr int tl = PAR ? 2 * u - 1 : 2 * u, th = tl + 1;
    v2u lo = {0, 0}, hi = {0, 0};
    if constexpr (tl >= 0) lo = pack4(xf[2 * u] + __builtin_bit_cast(f32x4, gv[2 * u]) * o[tl]);
    if constexpr (th <= 8) hi = pack4(xf[2 * u + 1] + __builtin_bit_cast(f32x4, gv[2 * u + 1]) * o[th]);
    x2[u] = cat(lo, hi);
    const int so = O_X2 + (qt * 36 + ks0 + u) * 1024;
    if constexpr (tl >= 0 && th <= 8) st16(x2[u], c.rs, vo, so);
    else if constexpr (tl >= 0) st8(lo, c.rs, vo, so);
    else st8(hi, c.rs, vo + 8, so); });
  waitl();
  sfor<25>([&](auto ic) { pin(wk[CV(ic) / 5][CV(ic) % 5]); });
  f32x4 s[6];
  sfor<5>([&](auto uc) { constexpr int u = CV(uc);
    sfor<5>([&](auto tc) { constexpr int t = CV(tc); s[t] = mfma(wk[u][t], x2[u], u == 0 ? f32x4{0, 0, 0, 0} : s[t]); });
    s[5] = mfma(x2[u], x2[u], u == 0 ? f32x4{0, 0, 0, 0} : s[5]); SB(); });
  sfor<6>([&](auto tc) { pin(s[CV(tc)]); });
  mfma_settle();
  sfor<5>([&](auto tc) { st16(__builtin_bit_cast(v4u, s[CV(tc)]), c.rs, vo, O_SCP + ((qt * 8 + nb) * 5 + CV(tc)) * 1024); });
  const int r = lane & 15, e = r & 3;
  if ((lane >> 4) == (r >> 2)) __builtin_amdgcn_raw_buffer_store_b32(asu(e == 0 ? s[5][0] : e == 1 ? s[5][1] : e == 2 ? s[5][2] : s[5][3]), c.rs, r * 4, O_LNP + (qt * 8 + nb) * 64, SC1);
}

template <int PAR> DEV void k4_out(v4u (&vp)[9][3], v4u (&pc)[3], v4u (&x2)[5], rsrc_t ro, int nb, int qt, int lane) {
  const int r = lane & 15, q = lane >> 4, ks0 = (9 * nb) >> 1;
  f32x4 o[9];
  sfor<9>([&](auto tc) { constexpr int t = CV(tc); o[t] = mfma(vp[t][2], pc[2], mfma(vp[t][1], pc[1], mfma(vp[t][0], pc[0], f32x4{0, 0, 0, 0}))); });
  const int oo = (qt * 16 + r) * (D * 2) + ks0 * 64 + q * 16;
  sfor<5>([&](auto uc) { constexpr int u = CV(uc); constexpr int tl = PAR ? 2 * u - 1 : 2 * u, th = tl + 1;
    v4u y = {0, 0, 0, 0};
    if constexpr (tl >= 0) { y[0] = pk2(bflo(x2[u][0]) + o[tl][0], bfhi(x2[u][0]) + o[tl][1]); y[1] = pk2(bflo(x2[u][1]) + o[tl][2], bfhi(x2[u][1]) + o[tl][3]); }
    if constexpr (th <= 8) { y[2] = pk2(bflo(x2[u][2]) + o[th][0], bfhi(x2[u][2]) + o[th][1]); y[3] = pk2(bflo(x2[u][3]) + o[th][2], bfhi(x2[u][3]) + o[th][3]); }
    if constexpr (tl >= 0 && th <= 8) st16o<u * 64>(y, ro, oo);
    else if constexpr (tl >= 0) __builtin_amdgcn_raw_buffer_store_b64(v2u{y[0], y[1]}, ro, oo, u * 64, 0);
    else __builtin_amdgcn_raw_buffer_store_b64(v2u{y[2], y[3]}, ro, oo + 8, u * 64, 0); });
}

template <int NT> DEV void tv_scores(const Ctx& c, int mi, int ci, f32x4 (&sacc)[2][4], v2u& pollv) {
  using P = P1<NT>;
  constexpr int NV = 9 - NT;
  constexpr Plan pl = P::make();
  static_assert(pl.ok, "schedule");
  const int lane = c.lane, w = c.w, vo = lane * 16, b = mi >> 3, qb = b * 16 + ci;
  v4u h[72];
  flag_spin(c, F_H + mi * 8 + 2 * w + (lane & 1));
  {
    const int hb = O_H + (mi * 4 + w) * (72 * 1024);
    auto ldh = [&](auto rc) { sfor<72>([&](auto ic) { constexpr int f = (CV(ic) + 18 * CV(rc)) % 72;
      if constexpr (f < 64) ald16a<(f & 3) * 1024>(h[f], vo, c.rs, hb + (f >> 2) * 4096); else ald16<(f & 3) * 1024>(h[f], vo, c.rs, hb + (f >> 2) * 4096);
      if constexpr (CV(ic) == 55) waitv<32>(); }); waitv<0>(); };
    if (ci & 2) { if (ci & 1) ldh(IC(3)); else ldh(IC(2)); } else { if (ci & 1) ldh(IC(1)); else ldh(IC(0)); }
  }
  STAMP(1, 1);
  const int t0 = NT == 4 ? ci * 4 : 48 + (ci - 12) * 6, v0 = NT == 4 ? ci * 5 : 60 + (ci - 12) * 3;
  const int l9 = lane < 18 ? lane : 0;
  const int fw1 = F_W + ((l9 < 9 ? t0 : t0 + NT - 1) >> 3) * 9 + l9 % 9;
  int pf[4];
  pf[0] = F_W + 81 + ((l9 < 9 ? v0 : v0 + NV - 1) >> 3) * 9 + l9 % 9;
  pf[1] = F_T + (qb >> 1) * 16 + (lane < 12 ? lane : 0);
  pf[2] = F_T + (qb >> 1) * 16 + 12 + (lane & 3);
  { const int nb = mi & 7, s0 = nb < 7 ? (9 * nb) / 5 : 13;
    pf[3] = lane < 24 ? F_V + (b * 16 + s0 + (lane >> 3)) * 8 + (lane & 7) : lane < 42 ? F_X + b * 18 + lane - 24 : lane < 60 ? F_G + lane - 42 : F_G; }
  const int sw = O_WT + ci * (324 * 1024) + w * 2048, st = O_T + qb * (144 * 1024) + w * 2048 - 41 * 8192;
  const unsigned mw = c.lb + w * 2048;
  unsigned rd[3];
  rd[0] = c.lb + vo; rd[1] = rd[0] + 49152; rd[2] = rd[0] + 98304;
  v4u bf[2][6];
  auto op = [&](auto oc) { constexpr int kind = pl.kind[CV(oc)], idx = pl.idx[CV(oc)];
    if constexpr (kind == OP_OCT) { const int so = (idx < 41 ? sw : st) + idx * 8192; dma_m0_h(mw + ((8 * idx) % 144) * 1024, vo, c.rs, so); dma_2nd_h(vo, c.rs, so); }
    else if constexpr (kind == OP_POLL) ald8<O_FL>(pollv, pf[idx] * 8, c.rs, 0); };
  auto rdb = [&](auto kc, auto jc) { constexpr int p = P::piece(CV(kc), CV(jc)) % 144; dsr16<(p % 48) * 1024>(bf[CV(kc) & 1][CV(jc)], rd[p / 48]); };
  flag_spin(c, fw1);
  STAMP(1, 2);
  sfor<pl.pre>(op);
  waitv<pl.cnt_pre>(); bar();
  sfor<NT>([&](auto jc) { rdb(IC(0), jc); });
  SB();
  f32x4 acc[2][6];
  sfor<108>([&](auto kc) {
    constexpr int k = CV(kc), ks = k % 36, NB = P::nb(k), NM = 2 * NB;
    if constexpr (k == 36) {
      STAMP(1, 3);
      mfma_settle();
      const int tb = O_T + (mi * 2 + (w >> 1)) * (144 * 1024) + (w & 1) * 2048 + (t0 >> 1) * 4096;
      sfor<2>([&](auto ic) { sfor<NT / 2>([&](auto uc) { constexpr int i = CV(ic), u = CV(uc); st16(pack8(acc[i][2 * u], acc[i][2 * u + 1]), c.rs, vo, tb + u * 4096 + i * 1024); }); });
      asm volatile("s_nop 1"); SB();
    }
    if constexpr (k == 72) {
      STAMP(1, 4);
      mfma_settle();
      sfor<NV>([&](auto jc) { constexpr int j = CV(jc); const int T = v0 + j, nb = T / 9;
        st16(pack8(acc[0][j], acc[1][j]), c.rs, vo, O_VT + (((b * 8 + nb) * 32 + (mi & 7) * 4 + w) * 9 + T - 9 * nb) * 1024); });
      asm volatile("s_nop 1"); SB();
    }
    waitlv<pl.cnt[k]>();
    if constexpr (P::isbar(k)) bar();
    if constexpr (k == P::G_W2 || k == P::G_T1 || k == P::G_T2) {
      pin(pollv);
      if (!flag_ok(c, pollv)) flag_spin(c, pf[k == P::G_W2 ? 0 : k == P::G_T1 ? 1 : 2]);
    }
    if constexpr (k == P::FL_T) { if (w == 0) flag_set(c, F_T + mi * 16 + ci); }
    if constexpr (k == P::FL_V) { if (w == 0) flag_set(c, F_V + (b * 16 + ci) * 8 + (mi & 7)); }
    sfor<NB>([&](auto jc) { pin(bf[k & 1][CV(jc)]); });
    SB();
    sfor<NM>([&](auto mc) {
      constexpr int m = CV(mc), j = m >> 1, i = m & 1, f = 2 * ks + i, no = pl.in1[k] - pl.in0[k];
      if constexpr (k < 72) mma_h<(f < 64), (k >= 36), ks == 0>(acc[i][j], h[f], bf[k & 1][j]);
      else mma_h<(f < 64), true, ks == 0>(sacc[i][j], h[f], bf[k & 1][j]);
      SB();
      if constexpr (k + 1 < 108) { constexpr int NR = P::nb(k + 1), span = NM - NM / 4;
        srange<lo_of(m, NR, span), lo_of(m + 1, NR, span)>([&](auto rc) { rdb(IC(k + 1), rc); SB(); }); }
      srange<mid_of(m, no, NM), mid_of(m + 1, no, NM)>([&](auto qc) { op(IC(pl.in0[k] + CV(qc))); SB(); });
    });
  });
  mfma_settle();
  waitv<0>();
  pin(pollv);
  if (!flag_ok(c, pollv)) flag_spin(c, pf[3]);
}

__global__ __launch_bounds__(256, 1) void fused(char* ws, const us* xin, const us* cin, const us* y, const us* wqkv, const us* wo, const us* wq_c,
                                                const us* wkv, const us* wo_c, const us* wmod, us* out) {
  extern __shared__ __attribute__((aligned(16))) char smem[];
  Ctx c;
  c.ws = ws; c.rs = mkrs(ws, WS_BYTES); c.lds = (LAS char*)smem; c.lb = (unsigned)(size_t)c.lds;
  c.lane = threadIdx.x & 63; c.w = __builtin_amdgcn_readfirstlane(threadIdx.x >> 6);
  const int lane = c.lane, w = c.w, vo = lane * 16;
  STAMP(0, 0);
  asm volatile("s_mov_b32 m0, %0\n\ts_nop 0\n\tbuffer_load_dword %1, %2, 0 offen sc1 lds" ::"s"(c.lb + LDS_BYTES - 512 - w * 256), "v"(O_EP + lane * 4), "s"(c.rs) : "memory");
  const int x = blockIdx.x & 7, l = blockIdx.x >> 3;
  {
    const int q = x & 3, hx = x >> 2, ni = (x & 1) ? 4 : 5, nj = (x & 2) ? 4 : 5, nw = ni * nj;
    if (l < nw) {
      const int ti = (x & 1) * 5 + l % ni, tj = ((x >> 1) & 1) * 5 + l / ni, mu = ((q == 0 ? 0 : q == 1 ? 25 : q == 2 ? 45 : 65) + l) * 2 + hx;
      if (x < 4) w_tile<0>(c, wqkv, wo, cin, wmod, ti, tj, l % ni, l / ni, mu); else w_tile<1>(c, wqkv, wo, cin, wmod, ti, tj, l % ni, l / ni, mu);
    } else {
      const int e = l - nw, nk = q == 0 ? 6 : q == 1 ? 10 : q == 2 ? 8 : 12;
      if (e < nk) {
        const int t = hx * 18 + (q == 0 ? 0 : q == 1 ? 3 : q == 2 ? 8 : 12) + (e >> 1), bb = e & 1, rot = (e + 3 * bb) % 9;
        const int rt = bb * 64 + 33 + t;
        v4u xv[1][9];
        ln_load<1>(c, xin, t < 31 ? rt : 128, xv);
        small_tile<2>(c, wkv, y, bb, t, rot);
        { const rsrc_t rq = mkrs(hx ? wo_c : wq_c, D * D * 2);
          const int tv = hx ? lane * (D * 2) + (t - 18) * 128 : (t * 64 + lane) * (D * 2), ts = hx ? 64 * (D * 2) : 128;
          sfor<3>([&](auto ic) { const int n = w + 4 * CV(ic); if (n < 9) touch(c.lb, tv, rq, ((2 * rot + n) % 18) * ts); }); }
        if (t < 31) ln_tiles<1>(c, rt, xv);
        STAMP(4, 0);
        if (hx) small_tile<1>(c, wo_c, y, bb, t - 18, rot); else small_tile<0>(c, wq_c, y, bb, t, rot);
      } else {
        const int a2 = (q == 0 ? 0 : q == 1 ? 1 : q == 2 ? 3 : 7) + e - nk, rt = hx * 64 + 3 * a2;
        v4u xv[3][9];
        ln_load<3>(c, xin, rt, xv);
        waitv<0>(); c.make_tag();
        ln_tiles<3>(c, rt, xv);
        if (a2 < 9) gate_strip(c, cin, wmod, a2 * 2 + hx);
      }
    }
  }
  STAMP(1, 0);
  c.t0 = __builtin_amdgcn_readfirstlane(c.t0); c.t1 = __builtin_amdgcn_readfirstlane(c.t1);
  const int mi = MAP48 ? (x & 3) * 4 + (l & 3) : (x & 1) * 8 + (l & 7), ci = MAP48 ? (x >> 2) * 8 + (l >> 2) : (x >> 1) * 4 + (l >> 3), b = mi >> 3, kb = mi & 7, qb = b * 16 + ci;
  f32x4 sacc[2][4];
  v2u pollv;
  if (ci < 12) tv_scores<4>(c, mi, ci, sacc, pollv); else tv_scores<6>(c, mi, ci, sacc, pollv);
  STAMP(1, 5);
  const int nb = kb, qt = qb * 4 + w;
  const int sv = O_VT + (b * 8 + nb) * (288 * 1024) + w * 2048, rot = (ci + nb) & 3;
  auto voct = [&](int o) { const int u = o + 9 * rot; return sv + (u >= 36 ? u - 36 : u) * 8192; };
  const unsigned mw = c.lb + w * 2048;
  {
    LAS float* red = (LAS float*)(c.lds + LDS_MISC);
    const float cs = 0.029462782549439476f * 1.4426950408889634f;
    float mx[4], sm[4];
    sfor<4>([&](auto jc) { constexpr int j = CV(jc);
      mx[j] = max3(max3(sacc[0][j][0], sacc[0][j][1], sacc[0][j][2]), max3(sacc[0][j][3], sacc[1][j][0], sacc[1][j][1]), max3(sacc[1][j][2], sacc[1][j][3], sacc[1][j][3])); });
    red[lane * 4 + w] = xred4(mx[0], mx[1], mx[2], mx[3], FMax{});
    lbar();
    const f32x4 own = *(const LAS f32x4*)(red + lane * 4);
    sfor<4>([&](auto jc) { constexpr int j = CV(jc);
      const f32x4 t = *(const LAS f32x4*)(red + (j * 16 + (lane & 15)) * 4);
      const float m = max4(t) * cs;
      f32x4 p0, p1;
      for (int r = 0; r < 4; ++r) { p0[r] = __builtin_amdgcn_exp2f(sacc[0][j][r] * cs - m); p1[r] = __builtin_amdgcn_exp2f(sacc[1][j][r] * cs - m); }
      st16(pack8(p0, p1), c.rs, vo, O_P + ((qb * 4 + j) * 32 + kb * 4 + w) * 1024);
      sm[j] = ((p0[0] + p0[1]) + (p0[2] + p0[3])) + ((p1[0] + p1[1]) + (p1[2] + p1[3])); });
    st8(v2u{asu(max4(own) * cs), asu(xred4(sm[0], sm[1], sm[2], sm[3], FAdd{}))}, c.rs,
        (lane >> 4) * 4096 + (lane & 15) * 8, O_ST + qb * 16384 + kb * 512 + w * 128);
    SB();
  }
  sfor<4>([&](auto oc) { const int so = voct(CV(oc)); dma_m0_h(mw + CV(oc) * 8192, vo, c.rs, so); dma_2nd_h(vo, c.rs, so); });
  waitv<8>();
  flag_set(c, F_P + qb * 32 + kb * 4 + w);
  STAMP(1, 6);
  v4u xv[5], gv[10];
  { const rsrc_t rx = mkrs(xin, 2048 * D * 2);
    const int ks0 = (9 * nb) >> 1;
    sfor<5>([&](auto uc) { ald16p<CV(uc) * 64>(xv[CV(uc)], (lane & 15) * (D * 2) + (lane >> 4) * 16, rx, qt * (16 * D * 2) + ks0 * 64); });
    sfor<10>([&](auto uc) { ald16<(CV(uc) >> 1) * 128 + (CV(uc) & 1) * 16>(gv[CV(uc)], (lane >> 4) * 32, c.rs, O_GATE + b * 4608 + ks0 * 128); }); }
  srange<4, 18>([&](auto oc) { const int so = voct(CV(oc)); dma_m0_h(mw + CV(oc) * 8192, vo, c.rs, so); dma_2nd_h(vo, c.rs, so); });
  flag_spin(c, F_P + qb * 32 + (lane & 31));
  STAMP(1, 7);
  f32x4 xf[10];
  sfor<5>([&](auto uc) { constexpr int u = CV(uc); pin(xv[u]);
    xf[2 * u] = f32x4{bflo(xv[u][0]), bfhi(xv[u][0]), bflo(xv[u][1]), bfhi(xv[u][1])}; xf[2 * u + 1] = f32x4{bflo(xv[u][2]), bfhi(xv[u][2]), bflo(xv[u][3]), bfhi(xv[u][3])}; });
  sfor<10>([&](auto uc) { pin(gv[CV(uc)]); });
  f32x4 tot[9];
  {
    constexpr Plan pl = P3::make();
    static_assert(pl.ok, "schedule");
    v2u sts[8];
    sfor<8>([&](auto ic) { ald8<CV(ic) * 128>(sts[CV(ic)], (lane >> 4) * 1024 + (lane & 15) * 8, c.rs, O_ST + qt * 4096); });
    const int sk = O_WKT + (b * 36 + ((9 * nb) >> 1)) * 5120 + w * 2048 - 36 * 8192, ab = O_P + qt * 32768;
    unsigned rd[3];
    rd[0] = c.lb + vo; rd[1] = rd[0] + 49152; rd[2] = rd[0] + 98304;
    v4u a[P3::AW], bf[2][9];
    f32x4 acc[9];
    float f[8];
    auto op = [&](auto oc) { constexpr int kind = pl.kind[CV(oc)], idx = pl.idx[CV(oc)];
      if constexpr (kind == OP_OCT) { const int so = idx < 36 ? voct(idx) : sk + idx * 8192; dma_m0_h(mw + ((8 * idx) % 144) * 1024, vo, c.rs, so); dma_2nd_h(vo, c.rs, so); }
      else ald16<(idx & 3) * 1024>(a[idx % P3::AW], vo, c.rs, ab + (((idx >> 2) + 2 * rot) & 7) * 4096); };
    auto rdb = [&](auto kc, auto jc) { constexpr int p = (CV(kc) * 9 + CV(jc)) % 144; dsr16<(p % 48) * 1024>(bf[CV(kc) & 1][CV(jc)], rd[p / 48]); };
    sfor<pl.pre>(op);
    bar();
    sfor<9>([&](auto jc) { rdb(IC(0), jc); });
    SB();
    sfor<32>([&](auto kc) {
      constexpr int k = CV(kc), no = pl.in1[k] - pl.in0[k];
      waitlv<pl.cnt[k]>();
      if constexpr (P3::isbar(k)) bar();
      sfor<9>([&](auto jc) { pin(bf[k & 1][CV(jc)]); });
      pin(a[k % P3::AW]);
      if constexpr (k == 1) {
        sfor<8>([&](auto ic) { pin(sts[CV(ic)]); });
        const float m0 = asf(sts[0][0]), m1 = asf(sts[4][0]), m = xmax16_32(fmaxf(m0, m1));
        const float e0 = __builtin_amdgcn_exp2f(m0 - m), e1 = __builtin_amdgcn_exp2f(m1 - m);
        const float s = __builtin_amdgcn_rcpf(xsum16_32(e0 * ((asf(sts[0][1]) + asf(sts[1][1])) + (asf(sts[2][1]) + asf(sts[3][1]))) + e1 * ((asf(sts[4][1]) + asf(sts[5][1])) + (asf(sts[6][1]) + asf(sts[7][1])))));
        for (int i = 0; i < 4; ++i) { f[2 * i] = __shfl(e0 * s, (lane & 15) + 16 * ((i + rot) & 3)); f[2 * i + 1] = __shfl(e1 * s, (lane & 15) + 16 * ((i + rot) & 3)); }
        waitl();
      }
      SB();
      sfor<9>([&](auto mc) {
        constexpr int m = CV(mc);
        if constexpr (k % 4 == 0 && k > 0) { if constexpr (k == 4) tot[m] = acc[m] * f[0]; else tot[m] += acc[m] * f[k / 4 - 1]; }
        acc[m] = mfma(bf[k & 1][m], a[k % P3::AW], k % 4 == 0 ? f32x4{0, 0, 0, 0} : acc[m]);
        SB();
        if constexpr (k + 1 < 32) srange<lo_of(m, 9, 7), lo_of(m + 1, 9, 7)>([&](auto rc) { rdb(IC(k + 1), rc); SB(); });
        srange<mid_of(m, no, 9), mid_of(m + 1, no, 9)>([&](auto qc) { op(IC(pl.in0[k] + CV(qc))); SB(); });
      });
    });
    sfor<9>([&](auto jc) { tot[CV(jc)] += acc[CV(jc)] * f[7]; });
  }
  STAMP(1, 8);
  waitv<0>(); bar();
  if (nb & 1) k3_epi<1>(c, tot, xf, gv, nb, qt); else k3_epi<0>(c, tot, xf, gv, nb, qt);
  const int qt4 = qb * 4 + (nb >> 1), nb4 = (nb & 1) * 4 + w;
  SB();
  v4u vp[9][3];
  flag_spin(c, F_X + 36 + b * 18 + (lane < 18 ? lane : 0));
  flag_set(c, F_S + qt * 8 + nb);
  STAMP(1, 9);
  sfor<27>([&](auto ic) { constexpr int i = CV(ic); ald16<(i & 3) * 1024>(vp[i / 3][i % 3], vo, c.rs, O_VPT + (b * 72 + 9 * nb4) * 3072 + (i >> 2) * 4096); });
  LAS float* ured = (LAS float*)(c.lds + LDS_MISC + 2048);
  { const int tid = w * 64 + lane;
    if (tid < 240) { const int g = tid / 80, m = tid % 80; float u = 0.f;
      for (int i = 0; i < 6; ++i) u += asf(__builtin_amdgcn_raw_buffer_load_b32(c.rs, (g * 6 + i) * 320 + m * 4, O_U + b * 5760, SC1));
      ured[tid] = u; } }
  if (blockIdx.x == 0 && w == 3) {
    sfor<4>([&](auto ic) { flag_spin(c, F_T + CV(ic) * 64 + lane); });
    if (lane == 0) __builtin_amdgcn_raw_buffer_store_b64(c.tag(), c.rs, 0, O_EP, SC1);
  }
  flag_spin(c, F_S + qt4 * 8 + (lane & 7));
  STAMP(1, 10);
  {
    const int r = lane & 15, q = lane >> 4;
    v4u sp[2][5], x2[5];
    float lp0, lp1;
    sfor<10>([&](auto ic) { constexpr int i = CV(ic); ald16<(i & 3) * 1024>(sp[i / 5][i % 5], vo, c.rs, O_SCP + (qt4 * 8 + 2 * w) * 5120 + (i >> 2) * 4096); });
    asm volatile("buffer_load_dword %0, %2, %3, %4 offen sc1\n\tbuffer_load_dword %1, %2, %3, %4 offen offset:64 sc1" : "=&v"(lp0), "=&v"(lp1) : "v"(q * 128 + r * 4), "s"(c.rs), "s"(O_LNP + qt4 * 512));
    sfor<5>([&](auto uc) { constexpr int u = CV(uc); ald16<(u & 3) * 1024>(x2[u], vo, c.rs, O_X2 + (qt4 * 36 + ((9 * nb4) >> 1)) * 1024 + (u >> 2) * 4096); });
    waitv<5>();
    sfor<10>([&](auto ic) { pin(sp[CV(ic) / 5][CV(ic) % 5]); });
    pin(lp0); pin(lp1);
    LAS f32x4* red = (LAS f32x4*)(c.lds + 32768);
    sfor<5>([&](auto tc) { constexpr int t = CV(tc); red[(w * 5 + t) * 64 + lane] = __builtin_bit_cast(f32x4, sp[0][t]) + __builtin_bit_cast(f32x4, sp[1][t]); });
    const float sq = xsum16_32(lp0 + lp1) * (1.f / D);
    lbar();
    f32x4 s[5];
    float mx = -3.0e38f;
    sfor<5>([&](auto tc) { constexpr int t = CV(tc); s[t] = (red[t * 64 + lane] + red[(5 + t) * 64 + lane]) + (red[(10 + t) * 64 + lane] + red[(15 + t) * 64 + lane]); });
    const float mean = xsum16_32(q == 3 ? s[4][1] : 0.f) * (1.f / D);
    const float cc = 0.029462782549439476f * 1.4426950408889634f * __builtin_amdgcn_rsqf(fmaxf(sq - mean * mean, 0.f) + 1e-5f);
    sfor<5>([&](auto tc) { constexpr int t = CV(tc);
      const LAS f32x4* u4 = (const LAS f32x4*)(ured + t * 16 + q * 4);
      s[t] = (s[t] - ((u4[0] + u4[20]) + u4[40]) * mean) * cc;
      if constexpr (t == 4) for (int e = 1; e < 4; ++e) s[t][e] = q == 3 ? -3.0e38f : s[t][e];
      mx = max3(max3(mx, s[t][0], s[t][1]), s[t][2], s[t][3]); });
    mx = xmax16_32(mx);
    float sum = 0.f;
    sfor<5>([&](auto tc) { constexpr int t = CV(tc); for (int e = 0; e < 4; ++e) { s[t][e] = __builtin_amdgcn_exp2f(s[t][e] - mx); sum += s[t][e]; } });
    sum = __builtin_amdgcn_rcpf(xsum16_32(sum));
    v4u pc[3];
    pc[0] = pack8(s[0] * sum, s[1] * sum); pc[1] = pack8(s[2] * sum, s[3] * sum); pc[2] = pack8(s[4] * sum, f32x4{0, 0, 0, 0});
    waitv<0>();
    sfor<27>([&](auto ic) { pin(vp[CV(ic) / 3][CV(ic) % 3]); });
    sfor<5>([&](auto uc) { pin(x2[CV(uc)]); });
    const rsrc_t ro = mkrs(out, 2048 * D * 2);
    if (w & 1) k4_out<1>(vp, pc, x2, ro, nb4, qt4, lane); else k4_out<0>(vp, pc, x2, ro, nb4, qt4, lane);
  }
}
}

extern "C" void kernel_launch(void* const* d_in, const int* in_sizes, int n_in, void* d_out, int out_size, void* d_ws, size_t ws_size, hipStream_t stream) {
  (void)hipFuncSetAttribute((const void*)fused, hipFuncAttributeMaxDynamicSharedMemorySize, LDS_BYTES);
  fused<<<256, 256, LDS_BYTES, stream>>>((char*)d_ws + (WSEND ? ((ws_size - WS_BYTES) & ~(size_t)0xfffff) : 0), (const us*)d_in[0], (const us*)d_in[1], (const us*)d_in[2], (const us*)d_in[3], (const us*)d_in[4],
                                         (const us*)d_in[5], (const us*)d_in[6], (const us*)d_in[7], (const us*)d_in[8], (us*)d_out);
}
```

```cpp
#define STAG 0
#define PREC 1
#define PRET 1
#include <hip/hip_runtime.h>
#include <utility>
#include <stdint.h>

#define NTPOL 1
#define WSEND 0
#define MAP48 0
namespace {
typedef unsigned v4u __attribute__((ext_vector_type(4)));
typedef unsigned v2u __attribute__((ext_vector_type(2)));
typedef float f32x4 __attribute__((ext_vector_type(4)));
typedef float f32x2 __attribute__((ext_vector_type(2)));
typedef short bf16x8 __attribute__((ext_vector_type(8)));
typedef __bf16 bf16x2_t __attribute__((ext_vector_type(2)));
typedef __amdgpu_buffer_rsrc_t rsrc_t;
typedef unsigned short us;
#define LAS __attribute__((address_space(3)))
#define DEV __device__ __forceinline__
#define SB() __builtin_amdgcn_sched_barrier(0)
#define IC(x) std::integral_constant<int, (x)>{}
#define CV(x) decltype(x)::value

template <int B, int... I, class F> DEV void sfor_impl(std::integer_sequence<int, I...>, F&& f) { (f(std::integral_constant<int, B + I>{}), ...); }
template <int N, class F> DEV void sfor(F&& f) { sfor_impl<0>(std::make_integer_sequence<int, N>{}, f); }
template <int B, int E, class F> DEV void srange(F&& f) { sfor_impl<B>(std::make_integer_sequence<int, (E > B ? E - B : 0)>{}, f); }

constexpr int D = 1152, KS = 36;
constexpr unsigned O_EP = 0;
constexpr unsigned O_FL = 256;
constexpr int F_MU = 0, F_G = 192, F_H = 256, F_W = 384, F_KVC = 576, F_X = 704, F_T = 1024, F_V = 1280, F_P = 1536, F_S = 2560;
constexpr unsigned O_GATE = 32768;
constexpr unsigned O_MODP = O_GATE + 16384;
constexpr unsigned O_H = O_MODP + 81920;
constexpr unsigned O_WT = O_H + 64 * 72 * 1024;
constexpr unsigned O_T = O_WT + (16 * 324 + 8) * 1024;
constexpr unsigned O_VT = O_T + 32 * 144 * 1024;
constexpr unsigned O_P = O_VT + 16 * 288 * 1024;
constexpr unsigned O_ST = O_P + 128 * 32 * 1024;
constexpr unsigned O_KC = O_ST + 128 * 4096;
constexpr unsigned O_VC = O_KC + 360 * 1024;
constexpr unsigned O_WKT = O_VC + 360 * 1024;
constexpr unsigned O_VPT = O_WKT + 368 * 1024;
constexpr unsigned O_X2 = O_VPT + 432 * 1024;
constexpr unsigned O_SCP = O_X2 + 128 * 36 * 1024;
constexpr unsigned O_LNP = O_SCP + 128 * 40 * 1024;
constexpr unsigned O_U = O_LNP + 128 * 1024;
constexpr unsigned WS_BYTES = O_U + 2 * 18 * 80 * 4;
constexpr int LDS_MISC = 147456, LDS_BYTES = 163840;
constexpr int SC1 = 16;

#define STAMP(k, s)

DEV f32x4 mfma(v4u a, v4u b, f32x4 c) {
  return __builtin_amdgcn_mfma_f32_16x16x32_bf16(__builtin_bit_cast(bf16x8, a), __builtin_bit_cast(bf16x8, b), c, 0, 0, 0);
}
DEV unsigned pk2(float a, float b) { f32x2 v = {a, b}; return __builtin_bit_cast(unsigned, __builtin_convertvector(v, bf16x2_t)); }
DEV float asf(unsigned u) { return __builtin_bit_cast(float, u); }
DEV unsigned asu(float f) { return __builtin_bit_cast(unsigned, f); }
DEV float bflo(unsigned u) { return __builtin_bit_cast(float, u << 16); }
DEV float bfhi(unsigned u) { return __builtin_bit_cast(float, u & 0xffff0000u); }
DEV v4u pack8(f32x4 a, f32x4 b) { return v4u{pk2(a[0], a[1]), pk2(a[2], a[3]), pk2(b[0], b[1]), pk2(b[2], b[3])}; }
DEV v2u pack4(f32x4 a) { return v2u{pk2(a[0], a[1]), pk2(a[2], a[3])}; }
DEV rsrc_t mkrs(const void* p, unsigned bytes) { return __builtin_amdgcn_make_buffer_rsrc((void*)p, 0, bytes, 0x00020000); }
DEV void dma_m0(unsigned m, int vo, rsrc_t rs, int so) {
  asm volatile("s_mov_b32 m0, %0\n\ts_nop 0\n\tbuffer_load_dwordx4 %1, %2, %3 offen lds" ::"s"(m), "v"(vo), "s"(rs), "s"(so) : "memory");
}
DEV void dma_m0_n(unsigned m, int vo, rsrc_t rs, int so) {
  if constexpr (NTPOL) asm volatile("s_mov_b32 m0, %0\n\ts_nop 0\n\tbuffer_load_dwordx4 %1, %2, %3 offen nt lds" ::"s"(m), "v"(vo), "s"(rs), "s"(so) : "memory");
  else dma_m0(m, vo, rs, so);
}
DEV void dma_m0_h(unsigned m, int vo, rsrc_t rs, int so) {
  asm volatile("s_mov_b32 m0, %0\n\ts_nop 0\n\tbuffer_load_dwordx4 %1, %2, %3 offen sc1 lds" ::"s"(m), "v"(vo), "s"(rs), "s"(so) : "memory");
}
DEV void dma_2nd_h(int vo, rsrc_t rs, int so) {
  asm volatile("buffer_load_dwordx4 %0, %1, %2 offen offset:1024 sc1 lds" ::"v"(vo), "s"(rs), "s"(so) : "memory");
}
template <int OFF> DEV void ald16(v4u& d, int vo, rsrc_t rs, int so) {
  asm volatile("buffer_load_dwordx4 %0, %1, %2, %3 offen offset:%4 sc1" : "=v"(d) : "v"(vo), "s"(rs), "s"(so), "i"(OFF));
}
template <int OFF> DEV void ald16p(v4u& d, int vo, rsrc_t rs, int so) {
  asm volatile("buffer_load_dwordx4 %0, %1, %2, %3 offen offset:%4" : "=v"(d) : "v"(vo), "s"(rs), "s"(so), "i"(OFF));
}
template <int OFF> DEV void ald16a(v4u& d, int vo, rsrc_t rs, int so) {
  asm volatile("buffer_load_dwordx4 %0, %1, %2, %3 offen offset:%4 sc1" : "=a"(d) : "v"(vo), "s"(rs), "s"(so), "i"(OFF));
}
template <int OFF> DEV void ald8(v2u& d, int vo, rsrc_t rs, int so) {
  asm volatile("buffer_load_dwordx2 %0, %1, %2, %3 offen offset:%4 sc1" : "=v"(d) : "v"(vo), "s"(rs), "s"(so), "i"(OFF));
}
template <int OFF> DEV void dsr16(v4u& d, unsigned addr) { asm volatile("ds_read_b128 %0, %1 offset:%2" : "=v"(d) : "v"(addr), "i"(OFF)); }
template <int OFF> DEV void dsrtr(v2u& d, unsigned addr) { asm volatile("ds_read_b64_tr_b16 %0, %1 offset:%2" : "=v"(d) : "v"(addr), "i"(OFF)); }
template <int N> DEV void waitv() { asm volatile("s_waitcnt vmcnt(%0)" ::"i"(N) : "memory"); }
template <int N> DEV void waitlv() { asm volatile("s_waitcnt vmcnt(%0) lgkmcnt(0)" ::"i"(N) : "memory"); }
DEV void waitl() { asm volatile("s_waitcnt lgkmcnt(0)" ::: "memory"); }
DEV void bar() { asm volatile("s_barrier" ::: "memory"); }
DEV void lbar() { asm volatile("s_waitcnt lgkmcnt(0)\n\ts_barrier" ::: "memory"); }
DEV void pin(v4u& x) { asm volatile("" : "+v"(x)); }
DEV void pin(v2u& x) { asm volatile("" : "+v"(x)); }
DEV void pin(int& x) { asm volatile("" : "+v"(x)); }
DEV void pin(float& x) { asm volatile("" : "+v"(x)); }
DEV void pin(f32x4& x) { asm volatile("" : "+v"(x)); }
DEV void pin(f32x2& x) { asm volatile("" : "+v"(x)); }
DEV void st16(v4u v, rsrc_t rs, int vo, int so) { asm volatile("buffer_store_dwordx4 %0, %1, %2, %3 offen sc1\n\ts_nop 1" ::"v"(v), "v"(vo), "s"(rs), "s"(so) : "memory"); }
template <int OFF> DEV void st16o(v4u v, rsrc_t rs, int vo) { asm volatile("buffer_store_dwordx4 %0, %1, %2, 0 offen offset:%3\n\ts_nop 1" ::"v"(v), "v"(vo), "s"(rs), "i"(OFF) : "memory"); }
DEV float max3(float a, float b, float c) { float r; asm("v_max3_f32 %0, %1, %2, %3" : "=v"(r) : "v"(a), "v"(b), "v"(c)); return r; }
DEV float max4(f32x4 a) { return max3(max3(a[0], a[1], a[2]), a[3], a[3]); }
DEV void st8(v2u v, rsrc_t rs, int vo, int so) { __builtin_amdgcn_raw_buffer_store_b64(v, rs, vo, so, SC1); }
DEV v4u ld16(rsrc_t rs, int vo, int so) { return __builtin_amdgcn_raw_buffer_load_b128(rs, vo, so, 0); }
DEV v4u ld16h(rsrc_t rs, int vo, int so) { return __builtin_amdgcn_raw_buffer_load_b128(rs, vo, so, SC1); }
struct FMax { DEV float operator()(float a, float b) const { return max3(a, b, b); } };
struct FAdd { DEV float operator()(float a, float b) const { return a + b; } };
template <class Op> DEV float sw32(float a, float b, Op op) { asm("s_nop 1\n\tv_permlane32_swap_b32 %0, %1" : "+v"(a), "+v"(b)); return op(a, b); }
template <class Op> DEV float sw16(float a, float b, Op op) { asm("s_nop 1\n\tv_permlane16_swap_b32 %0, %1" : "+v"(a), "+v"(b)); return op(a, b); }
DEV float xmax16_32(float v) { v = sw16(v, v, FMax{}); return sw32(v, v, FMax{}); }
DEV float xsum16_32(float v) { v = sw16(v, v, FAdd{}); return sw32(v, v, FAdd{}); }
template <class Op> DEV float xred4(float v0, float v1, float v2, float v3, Op op) { return sw16(sw32(v0, v2, op), sw32(v1, v3, op), op); }
DEV void touch(unsigned lb, int vo, rsrc_t rs, int so) {
  asm volatile("s_mov_b32 m0, %0\n\ts_nop 0\n\tbuffer_load_dword %1, %2, %3 offen lds" ::"s"(lb + LDS_BYTES - 256), "v"(vo), "s"(rs), "s"(so) : "memory");
}
template <bool HA, bool H_IS_A, bool FIRST> DEV void mma_h(f32x4& acc, const v4u& h, const v4u& o) {
#define MMA_(CH, X, Y) \
  if constexpr (FIRST) asm volatile("v_mfma_f32_16x16x32_bf16 %0, " X ", " Y ", 0" : "=&v"(acc) : CH(h), "v"(o)); \
  else asm volatile("v_mfma_f32_16x16x32_bf16 %0, " X ", " Y ", %0" : "+v"(acc) : CH(h), "v"(o));
  if constexpr (HA) { if constexpr (H_IS_A) { MMA_("a", "%1", "%2") } else { MMA_("a", "%2", "%1") } }
  else { if constexpr (H_IS_A) { MMA_("v", "%1", "%2") } else { MMA_("v", "%2", "%1") } }
#undef MMA_
}
DEV void mfma_settle() { asm volatile("s_nop 15" ::: "memory"); SB(); }

struct Ctx {
  char* ws; rsrc_t rs; unsigned lb; LAS char* lds; int lane, w; unsigned t0, t1;
  DEV void make_tag() {
    const v2u ep = *(volatile LAS v2u*)(lds + LDS_BYTES - 512 - w * 256);
    const unsigned long long t = (((unsigned long long)(unsigned)__builtin_amdgcn_readfirstlane(ep[1]) << 32) | (unsigned)__builtin_amdgcn_readfirstlane(ep[0])) + 0x9E3779B97F4A7C15ull;
    t0 = (unsigned)t; t1 = (unsigned)(t >> 32);
  }
  DEV v2u tag() const { return v2u{t0, t1}; }
};
DEV void flag_set(const Ctx& c, int f) { if (c.lane == 0) __builtin_amdgcn_raw_buffer_store_b64(c.tag(), c.rs, 0, O_FL + f * 8, SC1); }
DEV bool flag_ok(const Ctx& c, v2u v) { return __all(v[0] == c.t0 && v[1] == c.t1); }
DEV void flag_spin(const Ctx& c, int f) {
#pragma unroll 1
  for (int i = 0; i < 20000; ++i) {
    v2u v;
    asm volatile("buffer_load_dwordx2 %0, %1, %2, 0 offen offset:%3 sc1\n\ts_waitcnt vmcnt(0)" : "=&v"(v) : "v"(f * 8), "s"(c.rs), "i"(O_FL) : "memory");
    if (flag_ok(c, v)) return;
    __builtin_amdgcn_s_sleep(1);
  }
}
DEV void publish(const Ctx& c, int f) { waitv<0>(); bar(); if (c.w == 0) flag_set(c, f); }
DEV void publish_first(Ctx& c, int f) { waitv<0>(); c.make_tag(); bar(); if (c.w == 0) flag_set(c, f); }

constexpr int lo_of(int m, int n, int span) { int r = 0; while (r < n && (r * span) / n < m) ++r; return r; }
constexpr int mid_of(int m, int n, int nm) { int q = 0; while (q < n && ((2 * q + 1) * nm) / (2 * n) < m) ++q; return q; }
template <int ROW, int DS, int NTP, int NK, int NP, int NR, int NM, class Hook, class Issue, class Rd, class Pin, class Mm>
DEV void raw_loop(const Ctx& c, Hook&& hook, Issue&& issue, Rd&& rd, Pin&& pinf, Mm&& mm) {
  constexpr int NS = KS / NK;
  STAMP(ROW, 1);
  sfor<2>([&](auto sc) { sfor<NP>([&](auto pc) { issue(sc, pc); }); });
  hook(IC(-1));
  if constexpr (DS == 3) sfor<NP>([&](auto pc) { issue(IC(2), pc); });
  SB();
  STAMP(ROW, 2);
  waitv<(DS - 1) * NP + NTP>(); bar();
  STAMP(ROW, 3);
  sfor<NR>([&](auto rc) { rd(IC(0), rc); });
  SB();
  sfor<KS>([&](auto kc) {
    constexpr int k = CV(kc), s = k / NK, kk = k % NK;
    constexpr int p0 = (kk * NP) / NK, p1 = ((kk + 1) * NP) / NK, np = (s + DS < NS) ? p1 - p0 : 0;
    if constexpr (kk == NK - 1 && s + 1 < NS) { waitlv<(s + DS < NS ? p0 : 0) + (DS == 3 && s + 2 < NS ? NP : 0) + (s == 0 ? NTP : 0)>(); bar(); }
    else waitl();
    pinf(kc);
    hook(kc);
    SB();
    sfor<NM>([&](auto mc) {
      constexpr int m = CV(mc), span = NM - NM / 4;
      mm(kc, mc);
      SB();
      if constexpr (k + 1 < KS) srange<lo_of(m, NR, span), lo_of(m + 1, NR, span)>([&](auto rc) { rd(IC(k + 1), rc); SB(); });
      srange<mid_of(m, np, NM), mid_of(m + 1, np, NM)>([&](auto qc) { issue(IC(s + DS), IC(p0 + CV(qc))); SB(); });
    });
    if constexpr (k == 11) STAMP(ROW, 4);
    if constexpr (k == 23) STAMP(ROW, 5);
  });
  STAMP(ROW, 6);
}

DEV unsigned kc_rd(unsigned base, int lane, int h) { const int r = lane & 15, q = lane >> 4; return base + r * 128 + (((4 * h + q) ^ (r & 7)) << 4); }
template <bool PI> DEV int kc_src(int lane, int stride) { const int rr = lane >> 3; return (PI ? 8 * (rr >> 2) + (rr & 3) : rr) * stride + (((lane & 7) ^ rr) << 4); }
DEV int tr64_src(int lane, int w, int stride) { const int rr = lane >> 3; return rr * stride + (((lane & 7) ^ ((((rr >> 1) & 1) | ((w & 1) << 1)) << 1)) << 4); }
DEV unsigned tr64_rd(unsigned base, int lane, int w) {
  const int gg = lane >> 4, qq = (lane & 15) >> 2, pp = lane & 3;
  return base + (8 * gg + qq) * 128 + (((2 * w + (pp >> 1)) ^ (((qq >> 1) | ((gg & 1) << 1)) << 1)) << 4) + 8 * (pp & 1);
}
DEV int pi_inv_off(int t, int lane, int blk0, int blk1) {
  const int xx = 16 * (t & 1) + (lane & 15);
  return (((xx >> 2) & 1) ? blk1 : blk0) + (((lane >> 4) * 16 + 4 * (xx >> 3) + (xx & 3)) << 4);
}
DEV v4u cat(v2u a, v2u b) { return v4u{a[0], a[1], b[0], b[1]}; }
DEV v4u silu8(v4u v) {
  auto f = [](float x) { return x * __builtin_amdgcn_rcpf(1.f + __builtin_amdgcn_exp2f(x * -1.4426950408889634f)); };
  v4u o;
  for (int e = 0; e < 4; ++e) o[e] = pk2(f(bflo(v[e])), f(bfhi(v[e])));
  return o;
}

template <int TRB> DEV void w_tile(Ctx& c, const us* wqkv, const us* wo, const us* cin, const us* wmod, int ti, int tj, int pi, int pj, int mu) {
  const int lane = c.lane, w = c.w, wr = w >> 1, wc = w & 1;
  const rsrc_t ra = mkrs(wqkv, D * 3456 * 2), rb = TRB ? mkrs(wo, D * D * 2) : ra;
  const unsigned lb = c.lb;
  const int va = kc_src<true>(lane, 6912);
  const int sa = ti * 128 * 6912 + (TRB ? 4608 : 0) + (16 * (w & 1) + 4 * (w >> 1)) * 6912;
  int vb, sb;
  if constexpr (TRB) { vb = (lane >> 4) * 2304 + (((lane & 15) ^ (((lane >> 4) << 2) | w)) << 4); sb = tj * 256 + 4 * w * 2304; }
  else { vb = kc_src<false>(lane, 6912); sb = (tj * 128 + 8 * w) * 6912 + 2304; }
  unsigned ra_[2][2], rb_[2][8];
  sfor<2>([&](auto gc) { constexpr int g = CV(gc);
    sfor<2>([&](auto hc) { ra_[g][CV(hc)] = kc_rd(lb + g * 65536 + wr * 8192, lane, CV(hc)); });
    if constexpr (TRB) {
      const int gg = lane >> 4, qq = (lane & 15) >> 2, pp = lane & 3;
      sfor<8>([&](auto ec) { constexpr int cc = CV(ec) >> 1, t = CV(ec) & 1;
        const int row = 8 * gg + 4 * t + qq, ch = 2 * (4 * wc + cc) + (pp >> 1);
        rb_[g][CV(ec)] = lb + g * 65536 + 16384 + 256 * row + 16 * (ch ^ ((qq << 2) | ((2 * gg + t) & 3))) + 8 * (pp & 1); });
    } else sfor<2>([&](auto hc) { rb_[g][CV(hc)] = kc_rd(lb + g * 65536 + 16384 + wc * 8192, lane, CV(hc)); }); });
  const int so0 = STAG == 5 ? ((pi + pj) * 18 / 5) % 18 : STAG == 2 ? ((pi + pj) & 1) * 9 : 0;
  auto stg = [&](int st) { const int t = st + so0; return t >= 18 ? t - 18 : t; };
  int eo[4][2];
  const int ks = ti * 4 + wr * 2;
  auto blk = [&](int T, int v) {
    if constexpr (TRB) return T < 60 ? (T / 5) * 324 + 144 + (ks + v) * 5 + T % 5 : (12 + (T - 60) / 3) * 324 + 216 + (ks + v) * 3 + (T - 60) % 3;
    else return T < 48 ? (T >> 2) * 324 + (ks + v) * 4 + (T & 3) : (12 + (T - 48) / 6) * 324 + (ks + v) * 6 + (T - 48) % 6; };
  const int strip = mu >> 2, kq = mu & 3, dead = mu < 144 ? 0 : 0x7ff00000;
  v4u cv;
  ald16p<0>(cv, (lane < 36 && w < 2 ? lane * 16 : 0x7ff00000) | dead, mkrs(cin, 2 * D * 2), (w & 1) * 2304 + kq * 576);
  { const rsrc_t rw = mkrs(wmod, D * 6912 * 2);
    const int v64 = tr64_src(lane, w, 13824) | dead, s64 = strip * 128 + (kq * 288 + 8 * w) * 13824;
    sfor<9>([&](auto pc) { dma_m0_n(lb + 98304 + (w + 4 * CV(pc)) * 1024, v64, rw, s64 + CV(pc) * 32 * 13824); }); }
  v4u fa[2][4], fb[2][4];
  v2u ft[2][8];
  f32x4 acc[4][4];
  raw_loop<0, 3, 1, 2, 8, TRB ? 12 : 8, 16>(c,
    [&](auto kc) {
      if constexpr (CV(kc) == -1) {
        waitv<25>();
        pin(cv);
        LAS v4u* sc = (LAS v4u*)(c.lds + LDS_MISC);
        if (w < 2) { if (lane < 36) sc[w * 36 + lane] = silu8(cv); } else if (lane == 0) sc[72] = v4u{0, 0, 0, 0};
        waitlv<16>(); bar();
        const int r = lane & 15, q = lane >> 4;
        const unsigned cb = lb + LDS_MISC + (r < 2 ? r * 576 + q * 16 : 1152), cs = r < 2 ? 64 : 0, ra2 = tr64_rd(lb + 98304, lane, w);
        v2u mt[9][2]; v4u cf[9];
        sfor<9>([&](auto ic) { constexpr int i = CV(ic); dsrtr<i * 4096>(mt[i][0], ra2); dsrtr<i * 4096 + 512>(mt[i][1], ra2); dsr16<0>(cf[i], cb + i * cs); });
        waitl();
        f32x4 m = {0, 0, 0, 0};
        sfor<9>([&](auto ic) { constexpr int i = CV(ic); pin(mt[i][0]); pin(mt[i][1]); pin(cf[i]); m = mfma(cat(mt[i][0], mt[i][1]), cf[i], m); });
        pin(m); mfma_settle();
        st16(__builtin_bit_cast(v4u, m), c.rs, (r < 2 ? r * 36864 + q * 16 : 0x7ff00000) | dead, O_MODP + kq * 9216 + strip * 256 + w * 64);
      }
      if constexpr (CV(kc) == 3) { c.make_tag(); if (w == 0 && mu < 144) flag_set(c, F_MU + mu); }
      if constexpr (CV(kc) == -1) sfor<4>([&](auto jc) { constexpr int j = CV(jc); const int T0 = tj * 8 + wc * 4 + (j & 2);
      sfor<2>([&](auto vc) { constexpr int v = CV(vc); eo[j][v] = pi_inv_off(j, lane, blk(T0, v) * 1024, blk(T0 + 1, v) * 1024); pin(eo[j][v]); }); }); },
    [&](auto sc, auto pc) { constexpr int s = CV(sc), p = CV(pc), u = p >> 1;
      const unsigned m = lb + (s & 3) * 32768 + (p & 1) * 16384 + u * 4096 + w * 1024;
      if constexpr ((p & 1) == 0) dma_m0(m, va, ra, sa + u * 32 * 6912 + stg(s) * 128);
      else if constexpr (TRB) dma_m0(m, vb, rb, sb + u * 16 * 2304 + stg(s) * (64 * 2304));
      else dma_m0(m, vb, rb, sb + u * 32 * 6912 + stg(s) * 128); },
    [&](auto kc, auto rc) { constexpr int k = CV(kc), r = CV(rc), s = (k >> 1) & 3, h = k & 1;
      if constexpr (r < 4) dsr16<(s & 1) * 32768 + r * 2048>(fa[k & 1][r], ra_[s >> 1][h]);
      else if constexpr (TRB) dsrtr<(s & 1) * 32768 + h * 8192>(ft[k & 1][r - 4], rb_[s >> 1][r - 4]);
      else dsr16<(s & 1) * 32768 + (r - 4) * 2048>(fb[k & 1][r - 4], rb_[s >> 1][h]); },
    [&](auto kc) { constexpr int k = CV(kc);
      sfor<4>([&](auto ic) { pin(fa[k & 1][CV(ic)]); });
      if constexpr (TRB) sfor<4>([&](auto ic) { constexpr int i = CV(ic); pin(ft[k & 1][2 * i]); pin(ft[k & 1][2 * i + 1]); fb[k & 1][i] = cat(ft[k & 1][2 * i], ft[k & 1][2 * i + 1]); });
      else sfor<4>([&](auto ic) { pin(fb[k & 1][CV(ic)]); }); },
    [&](auto kc, auto mc) { constexpr int k = CV(kc), i = CV(mc) & 3, j = CV(mc) >> 2;
      acc[i][j] = mfma(fa[k & 1][i], fb[k & 1][j], k == 0 ? f32x4{0, 0, 0, 0} : acc[i][j]); });
  sfor<4>([&](auto jc) { constexpr int j = CV(jc);
    sfor<2>([&](auto vc) { constexpr int v = CV(vc); st16(pack8(acc[2 * v][j], acc[2 * v + 1][j]), c.rs, eo[j][v], O_WT); }); });
  STAMP(0, 7);
  publish(c, F_W + (TRB * 9 + tj) * 9 + ti);
  STAMP(0, 8);
}

template <int KIND> DEV void small_tile(Ctx& c, const us* wgt, const us* y, int b, int t, int rot) {
  constexpr bool TR = KIND != 0;
  constexpr int LD = KIND == 2 ? 4608 : D * 2;
  const int lane = c.lane, w = c.w;
  const rsrc_t r64 = mkrs(wgt, D * LD), r80 = KIND == 2 ? mkrs(y, 154 * D * 2) : c.rs;
  const int v64 = TR ? tr64_src(lane, w, LD) : kc_src<true>(lane, LD);
  const int s64 = TR ? t * 128 + 8 * w * LD : (t * 64 + 16 * (w & 1) + 4 * (w >> 1)) * LD;
  const int v80 = KIND == 2 ? kc_src<false>(lane, D * 2) : lane * 16, v80x = (KIND != 2 || (lane >> 3) < 5 || !(w & 1)) ? v80 : 0x7ff00000;
  const int s80 = KIND == 2 ? (b * 77 + (w & 1) * 40) * (D * 2) + (w >> 1) * 128 : (KIND ? O_VC : O_KC) + b * (180 * 1024) + w * 5120;
  auto stg = [&](int st) { const int u = st + rot; return u >= 9 ? u - 9 : u; };
  unsigned r64a[3][2], r80a[3][2];
  sfor<3>([&](auto gc) { constexpr int g = CV(gc); const unsigned base = c.lb + g * 49152;
    if constexpr (TR) r64a[g][0] = tr64_rd(base, lane, w); else sfor<2>([&](auto hc) { r64a[g][CV(hc)] = kc_rd(base + w * 2048, lane, CV(hc)); });
    if constexpr (KIND == 2) sfor<2>([&](auto hc) { r80a[g][CV(hc)] = kc_rd(base, lane, CV(hc)); }); else r80a[g][0] = base + lane * 16; });
  v4u f64[2], f80[2][5];
  v2u ft[2][2];
  f32x4 acc[5];
  if constexpr (KIND != 2) flag_spin(c, F_KVC + (KIND * 2 + b) * 18 + (lane < 18 ? lane : 0));
  raw_loop<KIND == 2 ? 2 : 4, 2, 0, 4, 9, TR ? 7 : 6, 5>(c, [](auto) {},
    [&](auto sc, auto pc) { constexpr int s = CV(sc), p = CV(pc);
      const unsigned m = c.lb + (s & 3) * 36864;
      if constexpr (p < 4) {
        if constexpr (TR) dma_m0(m + (w + 4 * p) * 1024, v64, r64, s64 + 32 * p * LD + stg(s) * (128 * LD));
        else dma_m0(m + (p >> 1) * 8192 + (w + 4 * (p & 1)) * 1024, v64, r64, s64 + 32 * (p & 1) * LD + (p >> 1) * 128 + stg(s) * 256);
      } else if constexpr (KIND == 2) dma_m0(m + 16384 + (5 * w + p - 4) * 1024, p == 8 ? v80x : v80, r80, s80 + (p - 4) * 8 * (D * 2) + stg(s) * 256);
      else dma_m0_h(m + 16384 + (5 * w + p - 4) * 1024, v80, r80, s80 + (p - 4) * 1024 + stg(s) * 20480); },
    [&](auto kc, auto rc) { constexpr int k = CV(kc), r = CV(rc), kk = k & 3, so = ((k >> 2) & 3) * 36864;
      if constexpr (r < 5) { constexpr int o = so + 16384 + (KIND == 2 ? (kk >> 1) * 10240 + r * 2048 : (kk * 5 + r) * 1024); dsr16<o % 49152>(f80[k & 1][r], r80a[o / 49152][KIND == 2 ? kk & 1 : 0]); }
      else if constexpr (TR) { constexpr int o = so + kk * 4096 + (r - 5) * 512; dsrtr<o % 49152>(ft[k & 1][r - 5], r64a[o / 49152][0]); }
      else { constexpr int o = so + (kk >> 1) * 8192; dsr16<o % 49152>(f64[k & 1], r64a[o / 49152][kk & 1]); } },
    [&](auto kc) { constexpr int k = CV(kc);
      sfor<5>([&](auto ic) { pin(f80[k & 1][CV(ic)]); });
      if constexpr (TR) { pin(ft[k & 1][0]); pin(ft[k & 1][1]); f64[k & 1] = cat(ft[k & 1][0], ft[k & 1][1]); } else pin(f64[k & 1]); },
    [&](auto kc, auto mc) { constexpr int k = CV(kc), m = CV(mc);
      const f32x4 z = k == 0 ? f32x4{0, 0, 0, 0} : acc[m];
      acc[m] = KIND == 1 ? mfma(f80[k & 1][m], f64[k & 1], z) : mfma(f64[k & 1], f80[k & 1][m], z); });
  if constexpr (KIND == 2) {
    const int isv = t >= 18, n0 = (t - 18 * isv) * 64 + 16 * w, q = lane >> 4;
    const int vo = ((2 * (w & 1) + (q >> 1)) * 16 + (lane & 15)) * 16 + (q & 1) * 8;
    sfor<5>([&](auto tc) { st8(pack4(acc[CV(tc)]), c.rs, vo, (isv ? O_VC : O_KC) + ((b * 36 + (n0 >> 5)) * 5 + CV(tc)) * 1024); });
    publish_first(c, F_KVC + (isv * 2 + b) * 18 + t - 18 * isv);
  } else if constexpr (KIND == 1) {
    const int T0 = b * 72 + t * 4 + (w & 2);
    const int off = pi_inv_off(w, lane, T0 * 3072, (T0 + 1) * 3072);
    st16(pack8(acc[0], acc[1]), c.rs, off, O_VPT);
    st16(pack8(acc[2], acc[3]), c.rs, off, O_VPT + 1024);
    st16(pack8(acc[4], f32x4{0, 0, 0, 0}), c.rs, off, O_VPT + 2048);
    publish(c, F_X + (2 + b) * 18 + t);
  } else {
    LAS float* red = (LAS float*)(c.lds + LDS_MISC);
    sfor<5>([&](auto tc) { v2u v = pack4(acc[CV(tc)]);
      if constexpr (CV(tc) == 4) { if ((lane & 15) == 13) v = v2u{0x3f803f80u, 0x3f803f80u}; }
      st8(v, c.rs, lane * 16 + (w & 1) * 8, O_WKT + ((b * 36 + 2 * t + (w >> 1)) * 5 + CV(tc)) * 1024);
      const float s = xsum16_32((bflo(v[0]) + bfhi(v[0])) + (bflo(v[1]) + bfhi(v[1])));
      if (lane < 16) red[w * 80 + CV(tc) * 16 + lane] = s; });
    lbar();
    const int tid = w * 64 + lane;
    if (tid < 80) __builtin_amdgcn_raw_buffer_store_b32(asu((red[tid] + red[80 + tid]) + (red[160 + tid] + red[240 + tid])), c.rs, tid * 4, O_U + (b * 18 + t) * 320, SC1);
    publish(c, F_X + b * 18 + t);
  }
}

DEV void gate_strip(Ctx& c, const us* cin, const us* wmod, int strip) {
  const int lane = c.lane, w = c.w, r = lane & 15, q = lane >> 4;
  const rsrc_t rw = mkrs(wmod, D * 6912 * 2), rc = mkrs(cin, 2 * D * 2);
  v4u cv, cv2;
  ald16p<0>(cv, lane * 16, rc, w * 1152);
  ald16p<1024>(cv2, lane < 8 ? lane * 16 : 0x7ff00000, rc, w * 1152);
  const int v64 = tr64_src(lane, w, 13824), s64 = 4608 + strip * 128 + 8 * w * 13824;
  sfor<36>([&](auto pc) { dma_m0_n(c.lb + (w + 4 * CV(pc)) * 1024, v64, rw, s64 + CV(pc) * 32 * 13824); });
  waitv<36>();
  pin(cv); pin(cv2);
  LAS v4u* sc = (LAS v4u*)(c.lds + LDS_MISC);
  sc[w * 72 + lane] = silu8(cv);
  if (lane < 8) sc[w * 72 + 64 + lane] = silu8(cv2);
  if (lane == 0) sc[288] = v4u{0, 0, 0, 0};
  const unsigned cb = c.lb + LDS_MISC + (r < 2 ? r * 2304 + q * 16 : 4608), cs = r < 2 ? 64 : 0;
  f32x4 acc = {0, 0, 0, 0};
  sfor<3>([&](auto gc) { constexpr int g = CV(gc);
    waitlv<24 - 12 * g>(); bar();
    const unsigned ra = tr64_rd(c.lb + g * 49152, lane, w);
    v2u ft[12][2]; v4u cf[12];
    sfor<12>([&](auto kc) { constexpr int k = CV(kc); dsrtr<k * 4096>(ft[k][0], ra); dsrtr<k * 4096 + 512>(ft[k][1], ra); dsr16<0>(cf[k], cb + (12 * g + k) * cs); });
    waitl();
    sfor<12>([&](auto kc) { constexpr int k = CV(kc); pin(ft[k][0]); pin(ft[k][1]); pin(cf[k]); acc = mfma(cat(ft[k][0], ft[k][1]), cf[k], acc); }); });
  const v2u pv = pack4(acc);
  if (r < 2) st16(v4u{pv[0] << 16, pv[0] & 0xffff0000u, pv[1] << 16, pv[1] & 0xffff0000u}, c.rs, r * 4608 + q * 16, O_GATE + strip * 256 + w * 64);
  publish(c, F_G + strip);
}

template <int N> DEV void ln_load(const Ctx& c, const us* xin, int rt0, v4u (&xv)[N][9]) {
  const rsrc_t rx = mkrs(xin, 2048 * D * 2);
  sfor<9 * N>([&](auto ic) { constexpr int n = CV(ic) / 9, k = CV(ic) % 9;
    ald16p<k * 64>(xv[n][k], (c.lane & 15) * (D * 2) + (c.lane >> 4) * 16, rx, (rt0 + n) * (16 * D * 2) + 9 * c.w * 64); });
}
template <int N> DEV void ln_tiles(const Ctx& c, int rt0, v4u (&xv)[N][9]) {
  const int lane = c.lane, w = c.w, r = lane & 15, q = lane >> 4;
  STAMP(3, 1);
  LAS f32x2* red = (LAS f32x2*)(c.lds + LDS_MISC + 8192);
  sfor<N>([&](auto nc) { constexpr int n = CV(nc);
    f32x4 s1 = {0, 0, 0, 0}, s2 = {0, 0, 0, 0};
    sfor<9>([&](auto kc) { constexpr int k = CV(kc); pin(xv[n][k]);
      s1 = mfma(v4u{0x3f803f80u, 0x3f803f80u, 0x3f803f80u, 0x3f803f80u}, xv[n][k], s1); s2 = mfma(xv[n][k], xv[n][k], s2); });
    const int e = r & 3;
    if (q == (r >> 2)) red[(n * 4 + w) * 16 + r] = f32x2{s1[0], e == 0 ? s2[0] : e == 1 ? s2[1] : e == 2 ? s2[2] : s2[3]}; });
  lbar();
  f32x2 nx[N][9][4];
  sfor<N>([&](auto nc) { constexpr int n = CV(nc);
    const f32x2 s = (red[n * 64 + r] + red[n * 64 + 16 + r]) + (red[n * 64 + 32 + r] + red[n * 64 + 48 + r]);
    const float mean = s[0] * (1.f / D), rs = __builtin_amdgcn_rsqf(fmaxf(s[1] * (1.f / D) - mean * mean, 0.f) + 1e-5f), nm = -mean * rs;
    sfor<9>([&](auto kc) { constexpr int k = CV(kc); for (int e = 0; e < 4; ++e) { nx[n][k][e] = f32x2{bflo(xv[n][k][e]), bfhi(xv[n][k][e])} * rs + nm; pin(nx[n][k][e]); } }); });
  STAMP(3, 2);
#pragma unroll 1
  for (int i = 0; i < 20000; ++i) {
    v2u v0, v1, v2;
    asm volatile("buffer_load_dwordx2 %0, %3, %4, 0 offen offset:%5 sc1\n\tbuffer_load_dwordx2 %1, %3, %4, 0 offen offset:%6 sc1\n\t"
                 "buffer_load_dwordx2 %2, %3, %4, 0 offen offset:%7 sc1\n\ts_waitcnt vmcnt(0)"
                 : "=&v"(v0), "=&v"(v1), "=&v"(v2) : "v"((lane < 48 ? lane : 0) * 8), "s"(c.rs), "i"(O_FL + F_MU * 8), "i"(O_FL + F_MU * 8 + 384), "i"(O_FL + F_MU * 8 + 768) : "memory");
    if (flag_ok(c, v0) && flag_ok(c, v1) && flag_ok(c, v2)) break;
    __builtin_amdgcn_s_sleep(1);
  }
  STAMP(3, 3);
  { v4u pv[3][4];
    const int mb = O_MODP + (rt0 >> 6) * 36864 + w * 1152;
    sfor<4>([&](auto uc) { constexpr int u = CV(uc);
      ald16<0>(pv[0][u], lane * 16, c.rs, mb + u * 9216); ald16<0>(pv[1][u], lane * 16, c.rs, mb + u * 9216 + 4608);
      ald16<1024>(pv[2][u], lane < 16 ? (lane & 7) * 16 + (lane >> 3) * 4608 : 0x7ff00000, c.rs, mb + u * 9216); });
    waitv<0>();
    LAS f32x4* ms = (LAS f32x4*)(c.lds + w * 2304);
    sfor<3>([&](auto ic) { constexpr int i = CV(ic);
      sfor<4>([&](auto uc) { pin(pv[i][CV(uc)]); });
      const v2u p = pack4((__builtin_bit_cast(f32x4, pv[i][0]) + __builtin_bit_cast(f32x4, pv[i][1])) + (__builtin_bit_cast(f32x4, pv[i][2]) + __builtin_bit_cast(f32x4, pv[i][3])));
      const f32x4 v = f32x4{bflo(p[0]), bfhi(p[0]), bflo(p[1]), bfhi(p[1])} + (i == 0 ? 0.f : i == 1 ? 1.f : (float)((lane >> 3) & 1));
      if constexpr (i < 2) ms[i * 72 + lane] = v; else if (lane < 16) ms[(lane >> 3) * 72 + 64 + (lane & 7)] = v; });
    waitl();
  }
  const unsigned ma = c.lb + w * 2304 + q * 32;
  v4u mv[2][4];
  sfor<4>([&](auto ic) { dsr16<(CV(ic) >> 1) * 1152 + (CV(ic) & 1) * 16>(mv[0][CV(ic)], ma); });
  sfor<9>([&](auto kc) { constexpr int k = CV(kc);
    waitl();
    sfor<4>([&](auto ic) { pin(mv[k & 1][CV(ic)]); });
    if constexpr (k + 1 < 9) sfor<4>([&](auto ic) { dsr16<(CV(ic) >> 1) * 1152 + (CV(ic) & 1) * 16 + (k + 1) * 128>(mv[(k + 1) & 1][CV(ic)], ma); });
    sfor<N>([&](auto nc) { constexpr int n = CV(nc);
      v4u o;
      sfor<4>([&](auto ec) { constexpr int e = CV(ec); const v4u t = mv[k & 1][e >> 1], a = mv[k & 1][2 + (e >> 1)];
        const f32x2 v = nx[n][k][e] * f32x2{asf(a[2 * (e & 1)]), asf(a[2 * (e & 1) + 1])} + f32x2{asf(t[2 * (e & 1)]), asf(t[2 * (e & 1) + 1])}; o[e] = pk2(v[0], v[1]); });
      st16(o, c.rs, lane * 16, O_H + ((((rt0 + n) >> 1) * 36 + 9 * w + k) * 2 + ((rt0 + n) & 1)) * 1024); });
    SB(); });
  STAMP(3, 4);
  waitv<0>(); bar();
  STAMP(3, 5);
  if (w == 0 && lane < N) __builtin_amdgcn_raw_buffer_store_b64(c.tag(), c.rs, lane * 8, O_FL + (F_H + rt0) * 8, SC1);
}

enum { OP_OCT, OP_A, OP_POLL, OP_ST };
struct Plan {
  static constexpr int MAXO = 200, MAXK = 110;
  int n = 0, kind[MAXO] = {}, idx[MAXO] = {}, wt[MAXO] = {}, need[MAXO] = {};
  int in0[MAXK] = {}, in1[MAXK] = {}, cnt[MAXK] = {}, pre = 0, cnt_pre = 0;
  bool ok = true;
  constexpr void add(int k, int i, int w, int nd) { kind[n] = k; idx[n] = i; wt[n] = w; need[n] = nd; ++n; }
  constexpr int left(int upto, int t) const {
    int last = -1, s = 0;
    for (int i = 0; i < upto; ++i) if (need[i] <= t) last = i;
    for (int i = last + 1; i < upto; ++i) s += wt[i];
    return s;
  }
};
template <int NT> struct P1 {
  static constexpr int NV = 9 - NT, NK = 108, NOCT = 59;
  static constexpr int G_W2 = 8, G_T1 = NT == 4 ? 65 : 61, G_T2 = 80, G_V = 108, FL_T = NT == 4 ? 44 : 48, FL_V = 80;
  static constexpr int nb(int k) { return k < 36 ? NT : k < 72 ? NV : 4; }
  static constexpr int piece(int k, int j) { return k < 36 ? k * NT + j : k < 72 ? 36 * NT + (k - 36) * NV + j : 328 + (k - 72) * 4 + j; }
  static constexpr int kof(int p) { return p < 36 * NT ? p / NT : p < 324 ? 36 + (p - 36 * NT) / NV : p < 328 ? 71 : 72 + (p - 328) / 4; }
  static constexpr bool isbar(int k) { return k % 4 == 0 || k < 4 || (k >= 70 && k <= 75); }
  static constexpr Plan make() {
    Plan p;
    auto need = [](int o) { int k = kof(8 * o) - 1; while (k >= 0 && !isbar(k)) --k; return k; };
    auto gate = [](int o) { return o >= 53 ? G_T2 : o >= 41 ? G_T1 : 8 * o + 7 >= 36 * NT ? G_W2 : 0; };
    int o = 0;
    for (; o < 3; ++o) p.add(OP_OCT, o, 2, need(o));
    p.pre = p.n; p.cnt_pre = p.left(p.n, -1);
    for (int k = 0; k < NK; ++k) {
      if (k == 36) p.add(OP_ST, 0, NT, FL_T);
      if (k == 72) p.add(OP_ST, 1, NV, FL_V);
      p.cnt[k] = p.left(p.n, k);
      p.in0[k] = p.n;
      if (k == G_W2 - 4) p.add(OP_POLL, 0, 1, G_W2);
      if (k == G_T1 - 5) p.add(OP_POLL, 1, 1, G_T1);
      if (k == G_T2 - 5) p.add(OP_POLL, 2, 1, G_T2);
      if (k == G_V - 6) p.add(OP_POLL, 3, 1, G_V);
      if (o < NOCT && gate(o) <= k) {
        bool free_ = o < 18;
        if (!free_) for (int kb = kof(8 * (o - 18) + 7); kb <= k; ++kb) if (isbar(kb)) free_ = true;
        if (free_) { if (need(o) <= k) p.ok = false; p.add(OP_OCT, o, 2, need(o)); ++o; }
      }
      p.in1[k] = p.n;
      if (p.cnt[k] > 60) p.ok = false;
    }
    if (o != NOCT) p.ok = false;
    return p;
  }
};
struct P3 {
  static constexpr int NK = 32, DIST = 5, AW = 7;
  static constexpr bool isbar(int k) { return k % 2 == 1; }
  static constexpr Plan make() {
    Plan p;
    for (int k = 0; k < DIST; ++k) p.add(OP_A, k, 1, k);
    p.pre = p.n;
    int o = 18;
    for (int k = 0; k < NK; ++k) {
      p.cnt[k] = p.left(p.n, k);
      p.in0[k] = p.n;
      if (k + DIST < NK) p.add(OP_A, k + DIST, 1, k + DIST);
      if (o < 40) {
        bool free_ = false;
        for (int kb = (8 * (o - 18) + 7) / 9; kb <= k; ++kb) if (isbar(kb)) free_ = true;
        int nd = o < 36 ? (8 * o) / 9 - 1 : NK;
        while (nd < NK && !isbar(nd)) --nd;
        if (free_) { if (nd <= k) p.ok = false; p.add(OP_OCT, o, 2, nd); ++o; }
      }
      p.in1[k] = p.n;
    }
    if (o != 40) p.ok = false;
    return p;
  }
};

template <int PAR> DEV void k3_epi(const Ctx& c, f32x4 (&o)[9], f32x4 (&xf)[10], v4u (&gv)[10], int nb, int qt) {
  const int lane = c.lane, vo = lane * 16, ks0 = (9 * nb) >> 1;
  v4u wk[5][5];
  sfor<25>([&](auto ic) { dsr16<CV(ic) * 1024>(wk[CV(ic) / 5][CV(ic) % 5], c.lb + vo); });
  v4u x2[5];
  sfor<5>([&](auto uc) { constexpr int u = CV(uc);
    constexpr int tl = PAR ? 2 * u - 1 : 2 * u, th = tl + 1;
    v2u lo = {0, 0}, hi = {0, 0};
    if constexpr (tl >= 0) lo = pack4(xf[2 * u] + __builtin_bit_cast(f32x4, gv[2 * u]) * o[tl]);
    if constexpr (th <= 8) hi = pack4(xf[2 * u + 1] + __builtin_bit_cast(f32x4, gv[2 * u + 1]) * o[th]);
    x2[u] = cat(lo, hi);
    const int so = O_X2 + (qt * 36 + ks0 + u) * 1024;
    if constexpr (tl >= 0 && th <= 8) st16(x2[u], c.rs, vo, so);
    else if constexpr (tl >= 0) st8(lo, c.rs, vo, so);
    else st8(hi, c.rs, vo + 8, so); });
  waitl();
  sfor<25>([&](auto ic) { pin(wk[CV(ic) / 5][CV(ic) % 5]); });
  f32x4 s[6];
  sfor<5>([&](auto uc) { constexpr int u = CV(uc);
    sfor<5>([&](auto tc) { constexpr int t = CV(tc); s[t] = mfma(wk[u][t], x2[u], u == 0 ? f32x4{0, 0, 0, 0} : s[t]); });
    s[5] = mfma(x2[u], x2[u], u == 0 ? f32x4{0, 0, 0, 0} : s[5]); SB(); });
  sfor<6>([&](auto tc) { pin(s[CV(tc)]); });
  mfma_settle();
  sfor<5>([&](auto tc) { st16(__builtin_bit_cast(v4u, s[CV(tc)]), c.rs, vo, O_SCP + ((qt * 8 + nb) * 5 + CV(tc)) * 1024); });
  const int r = lane & 15, e = r & 3;
  if ((lane >> 4) == (r >> 2)) __builtin_amdgcn_raw_buffer_store_b32(asu(e == 0 ? s[5][0] : e == 1 ? s[5][1] : e == 2 ? s[5][2] : s[5][3]), c.rs, r * 4, O_LNP + (qt * 8 + nb) * 64, SC1);
}

template <int PAR> DEV void k4_out(v4u (&vp)[9][3], v4u (&pc)[3], v4u (&x2)[5], rsrc_t ro, int nb, int qt, int lane) {
  const int r = lane & 15, q = lane >> 4, ks0 = (9 * nb) >> 1;
  f32x4 o[9];
  sfor<9>([&](auto tc) { constexpr int t = CV(tc); o[t] = mfma(vp[t][2], pc[2], mfma(vp[t][1], pc[1], mfma(vp[t][0], pc[0], f32x4{0, 0, 0, 0}))); });
  const int oo = (qt * 16 + r) * (D * 2) + ks0 * 64 + q * 16;
  sfor<5>([&](auto uc) { constexpr int u = CV(uc); constexpr int tl = PAR ? 2 * u - 1 : 2 * u, th = tl + 1;
    v4u y = {0, 0, 0, 0};
    if constexpr (tl >= 0) { y[0] = pk2(bflo(x2[u][0]) + o[tl][0], bfhi(x2[u][0]) + o[tl][1]); y[1] = pk2(bflo(x2[u][1]) + o[tl][2], bfhi(x2[u][1]) + o[tl][3]); }
    if constexpr (th <= 8) { y[2] = pk2(bflo(x2[u][2]) + o[th][0], bfhi(x2[u][2]) + o[th][1]); y[3] = pk2(bflo(x2[u][3]) + o[th][2], bfhi(x2[u][3]) + o[th][3]); }
    if constexpr (tl >= 0 && th <= 8) st16o<u * 64>(y, ro, oo);
    else if constexpr (tl >= 0) __builtin_amdgcn_raw_buffer_store_b64(v2u{y[0], y[1]}, ro, oo, u * 64, 0);
    else __builtin_amdgcn_raw_buffer_store_b64(v2u{y[2], y[3]}, ro, oo + 8, u * 64, 0); });
}

template <int NT> DEV void tv_scores(const Ctx& c, int mi, int ci, f32x4 (&sacc)[2][4], v2u& pollv) {
  using P = P1<NT>;
  constexpr int NV = 9 - NT;
  constexpr Plan pl = P::make();
  static_assert(pl.ok, "schedule");
  const int lane = c.lane, w = c.w, vo = lane * 16, b = mi >> 3, qb = b * 16 + ci;
  v4u h[72];
  flag_spin(c, F_H + mi * 8 + 2 * w + (lane & 1));
  {
    const int hb = O_H + (mi * 4 + w) * (72 * 1024);
    auto ldh = [&](auto rc) { sfor<72>([&](auto ic) { constexpr int f = (CV(ic) + 18 * CV(rc)) % 72;
      if constexpr (f < 64) ald16a<(f & 3) * 1024>(h[f], vo, c.rs, hb + (f >> 2) * 4096); else ald16<(f & 3) * 1024>(h[f], vo, c.rs, hb + (f >> 2) * 4096);
      if constexpr (CV(ic) == 55) waitv<32>(); }); waitv<0>(); };
    if (ci & 2) { if (ci & 1) ldh(IC(3)); else ldh(IC(2)); } else { if (ci & 1) ldh(IC(1)); else ldh(IC(0)); }
  }
  STAMP(1, 1);
  const int t0 = NT == 4 ? ci * 4 : 48 + (ci - 12) * 6, v0 = NT == 4 ? ci * 5 : 60 + (ci - 12) * 3;
  const int l9 = lane < 18 ? lane : 0;
  const int fw1 = F_W + ((l9 < 9 ? t0 : t0 + NT - 1) >> 3) * 9 + l9 % 9;
  int pf[4];
  pf[0] = F_W + 81 + ((l9 < 9 ? v0 : v0 + NV - 1) >> 3) * 9 + l9 % 9;
  pf[1] = F_T + (qb >> 1) * 16 + (lane < 12 ? lane : 0);
  pf[2] = F_T + (qb >> 1) * 16 + 12 + (lane & 3);
  { const int nb = mi & 7, s0 = nb < 7 ? (9 * nb) / 5 : 13;
    pf[3] = lane < 24 ? F_V + (b * 16 + s0 + (lane >> 3)) * 8 + (lane & 7) : lane < 42 ? F_X + b * 18 + lane - 24 : lane < 60 ? F_G + lane - 42 : F_G; }
  const int sw = O_WT + ci * (324 * 1024) + w * 2048, st = O_T + qb * (144 * 1024) + w * 2048 - 41 * 8192;
  const unsigned mw = c.lb + w * 2048;
  unsigned rd[3];
  rd[0] = c.lb + vo; rd[1] = rd[0] + 49152; rd[2] = rd[0] + 98304;
  v4u bf[2][6];
  auto op = [&](auto oc) { constexpr int kind = pl.kind[CV(oc)], idx = pl.idx[CV(oc)];
    if constexpr (kind == OP_OCT) { const int so = (idx < 41 ? sw : st) + idx * 8192; dma_m0_h(mw + ((8 * idx) % 144) * 1024, vo, c.rs, so); dma_2nd_h(vo, c.rs, so); }
    else if constexpr (kind == OP_POLL) ald8<O_FL>(pollv, pf[idx] * 8, c.rs, 0); };
  auto rdb = [&](auto kc, auto jc) { constexpr int p = P::piece(CV(kc), CV(jc)) % 144; dsr16<(p % 48) * 1024>(bf[CV(kc) & 1][CV(jc)], rd[p / 48]); };
  flag_spin(c, fw1);
  STAMP(1, 2);
  sfor<pl.pre>(op);
  waitv<pl.cnt_pre>(); bar();
  sfor<NT>([&](auto jc) { rdb(IC(0), jc); });
  SB();
  f32x4 acc[2][6];
  sfor<108>([&](auto kc) {
    constexpr int k = CV(kc), ks = k % 36, NB = P::nb(k), NM = 2 * NB;
    if constexpr (k == 36) {
      STAMP(1, 3);
      mfma_settle();
      const int tb = O_T + (mi * 2 + (w >> 1)) * (144 * 1024) + (w & 1) * 2048 + (t0 >> 1) * 4096;
      sfor<2>([&](auto ic) { sfor<NT / 2>([&](auto uc) { constexpr int i = CV(ic), u = CV(uc); st16(pack8(acc[i][2 * u], acc[i][2 * u + 1]), c.rs, vo, tb + u * 4096 + i * 1024); }); });
      asm volatile("s_nop 1"); SB();
    }
    if constexpr (k == 72) {
      STAMP(1, 4);
      mfma_settle();
      sfor<NV>([&](auto jc) { constexpr int j = CV(jc); const int T = v0 + j, nb = T / 9;
        st16(pack8(acc[0][j], acc[1][j]), c.rs, vo, O_VT + (((b * 8 + nb) * 32 + (mi & 7) * 4 + w) * 9 + T - 9 * nb) * 1024); });
      asm volatile("s_nop 1"); SB();
    }
    waitlv<pl.cnt[k]>();
    if constexpr (P::isbar(k)) bar();
    if constexpr (k == P::G_W2 || k == P::G_T1 || k == P::G_T2) {
      pin(pollv);
      if (!flag_ok(c, pollv)) flag_spin(c, pf[k == P::G_W2 ? 0 : k == P::G_T1 ? 1 : 2]);
    }
    if constexpr (k == P::FL_T) { if (w == 0) flag_set(c, F_T + mi * 16 + ci); }
    if constexpr (k == P::FL_V) { if (w == 0) flag_set(c, F_V + (b * 16 + ci) * 8 + (mi & 7)); }
    sfor<NB>([&](auto jc) { pin(bf[k & 1][CV(jc)]); });
    SB();
    sfor<NM>([&](auto mc) {
      constexpr int m = CV(mc), j = m >> 1, i = m & 1, f = 2 * ks + i, no = pl.in1[k] - pl.in0[k];
      if constexpr (k < 72) mma_h<(f < 64), (k >= 36), ks == 0>(acc[i][j], h[f], bf[k & 1][j]);
      else mma_h<(f < 64), true, ks == 0>(sacc[i][j], h[f], bf[k & 1][j]);
      SB();
      if constexpr (k + 1 < 108) { constexpr int NR = P::nb(k + 1), span = NM - NM / 4;
        srange<lo_of(m, NR, span), lo_of(m + 1, NR, span)>([&](auto rc) { rdb(IC(k + 1), rc); SB(); }); }
      srange<mid_of(m, no, NM), mid_of(m + 1, no, NM)>([&](auto qc) { op(IC(pl.in0[k] + CV(qc))); SB(); });
    });
  });
  mfma_settle();
  waitv<0>();
  pin(pollv);
  if (!flag_ok(c, pollv)) flag_spin(c, pf[3]);
}

__global__ __launch_bounds__(256, 1) void fused(char* ws, const us* xin, const us* cin, const us* y, const us* wqkv, const us* wo, const us* wq_c,
                                                const us* wkv, const us* wo_c, const us* wmod, us* out) {
  extern __shared__ __attribute__((aligned(16))) char smem[];
  Ctx c;
  c.ws = ws; c.rs = mkrs(ws, WS_BYTES); c.lds = (LAS char*)smem; c.lb = (unsigned)(size_t)c.lds;
  c.lane = threadIdx.x & 63; c.w = __builtin_amdgcn_readfirstlane(threadIdx.x >> 6);
  const int lane = c.lane, w = c.w, vo = lane * 16;
  STAMP(0, 0);
  asm volatile("s_mov_b32 m0, %0\n\ts_nop 0\n\tbuffer_load_dword %1, %2, 0 offen sc1 lds" ::"s"(c.lb + LDS_BYTES - 512 - w * 256), "v"(O_EP + lane * 4), "s"(c.rs) : "memory");
  const int x = blockIdx.x & 7, l = blockIdx.x >> 3;
  if constexpr (PREC) {
    unsigned long long pc;
    asm volatile("s_getpc_b64 %0" : "=s"(pc));
    if (w == 3) touch(c.lb, (l * 64 + lane) * 128, mkrs((const void*)pc, 0x1f000), 0);
  }
  if constexpr (PRET) {
    const int o = (l * 64 + lane) << 16;
    if (w == 0) touch(c.lb, ((l * 19 + lane) << 16) | (lane < 19 ? 0 : 0x7ff00000), c.rs, 0);
    if (w == 1) { if (l == 0 || l == 1) touch(c.lb, o, mkrs(out, 2048 * D * 2), 0); else if (l < 4) touch(c.lb, o - (128 << 16), mkrs(xin, 2048 * D * 2), 0);
      else if (l < 6) touch(c.lb, o - (256 << 16), mkrs(wqkv, D * 3456 * 2), 0); else if (l == 6) touch(c.lb, lane << 16, mkrs(wo, D * D * 2), 0);
      else if (l == 7) touch(c.lb, lane << 16, mkrs(wq_c, D * D * 2), 0); else if (l == 8) touch(c.lb, lane << 16, mkrs(wo_c, D * D * 2), 0);
      else if (l < 11) touch(c.lb, o - (576 << 16), mkrs(wkv, D * 2304 * 2), 0); else if (l < 15) touch(c.lb, o - (704 << 16), mkrs(wmod, D * 6912 * 2), 0); }
  }
  {
    const int q = x & 3, hx = x >> 2, ni = (x & 1) ? 4 : 5, nj = (x & 2) ? 4 : 5, nw = ni * nj;
    if (l < nw) {
      const int ti = (x & 1) * 5 + l % ni, tj = ((x >> 1) & 1) * 5 + l / ni, mu = ((q == 0 ? 0 : q == 1 ? 25 : q == 2 ? 45 : 65) + l) * 2 + hx;
      if (x < 4) w_tile<0>(c, wqkv, wo, cin, wmod, ti, tj, l % ni, l / ni, mu); else w_tile<1>(c, wqkv, wo, cin, wmod, ti, tj, l % ni, l / ni, mu);
    } else {
      const int e = l - nw, nk = q == 0 ? 6 : q == 1 ? 10 : q == 2 ? 8 : 12;
      if (e < nk) {
        const int t = hx * 18 + (q == 0 ? 0 : q == 1 ? 3 : q == 2 ? 8 : 12) + (e >> 1), bb = e & 1, rot = (e + 3 * bb) % 9;
        const int rt = bb * 64 + 33 + t;
        v4u xv[1][9];
        ln_load<1>(c, xin, t < 31 ? rt : 128, xv);
        small_tile<2>(c, wkv, y, bb, t, rot);
        { const rsrc_t rq = mkrs(hx ? wo_c : wq_c, D * D * 2);
          const int tv = hx ? lane * (D * 2) + (t - 18) * 128 : (t * 64 + lane) * (D * 2), ts = hx ? 64 * (D * 2) : 128;
          sfor<3>([&](auto ic) { const int n = w + 4 * CV(ic); if (n < 9) touch(c.lb, tv, rq, ((2 * rot + n) % 18) * ts); }); }
        if (t < 31) ln_tiles<1>(c, rt, xv);
        STAMP(4, 0);
        if (hx) small_tile<1>(c, wo_c, y, bb, t - 18, rot); else small_tile<0>(c, wq_c, y, bb, t, rot);
      } else {
        const int a2 = (q == 0 ? 0 : q == 1 ? 1 : q == 2 ? 3 : 7) + e - nk, rt = hx * 64 + 3 * a2;
        v4u xv[3][9];
        ln_load<3>(c, xin, rt, xv);
        waitv<0>(); c.make_tag();
        ln_tiles<3>(c, rt, xv);
        if (a2 < 9) gate_strip(c, cin, wmod, a2 * 2 + hx);
      }
    }
  }
  STAMP(1, 0);
  c.t0 = __builtin_amdgcn_readfirstlane(c.t0); c.t1 = __builtin_amdgcn_readfirstlane(c.t1);
  const int mi = MAP48 ? (x & 3) * 4 + (l & 3) : (x & 1) * 8 + (l & 7), ci = MAP48 ? (x >> 2) * 8 + (l >> 2) : (x >> 1) * 4 + (l >> 3), b = mi >> 3, kb = mi & 7, qb = b * 16 + ci;
  f32x4 sacc[2][4];
  v2u pollv;
  if (ci < 12) tv_scores<4>(c, mi, ci, sacc, pollv); else tv_scores<6>(c, mi, ci, sacc, pollv);
  STAMP(1, 5);
  const int nb = kb, qt = qb * 4 + w;
  const int sv = O_VT + (b * 8 + nb) * (288 * 1024) + w * 2048, rot = (ci + nb) & 3;
  auto voct = [&](int o) { const int u = o + 9 * rot; return sv + (u >= 36 ? u - 36 : u) * 8192; };
  const unsigned mw = c.lb + w * 2048;
  {
    LAS float* red = (LAS float*)(c.lds + LDS_MISC);
    const float cs = 0.029462782549439476f * 1.4426950408889634f;
    float mx[4], sm[4];
    sfor<4>([&](auto jc) { constexpr int j = CV(jc);
      mx[j] = max3(max3(sacc[0][j][0], sacc[0][j][1], sacc[0][j][2]), max3(sacc[0][j][3], sacc[1][j][0], sacc[1][j][1]), max3(sacc[1][j][2], sacc[1][j][3], sacc[1][j][3])); });
    red[lane * 4 + w] = xred4(mx[0], mx[1], mx[2], mx[3], FMax{});
    lbar();
    const f32x4 own = *(const LAS f32x4*)(red + lane * 4);
    sfor<4>([&](auto jc) { constexpr int j = CV(jc);
      const f32x4 t = *(const LAS f32x4*)(red + (j * 16 + (lane & 15)) * 4);
      const float m = max4(t) * cs;
      f32x4 p0, p1;
      for (int r = 0; r < 4; ++r) { p0[r] = __builtin_amdgcn_exp2f(sacc[0][j][r] * cs - m); p1[r] = __builtin_amdgcn_exp2f(sacc[1][j][r] * cs - m); }
      st16(pack8(p0, p1), c.rs, vo, O_P + ((qb * 4 + j) * 32 + kb * 4 + w) * 1024);
      sm[j] = ((p0[0] + p0[1]) + (p0[2] + p0[3])) + ((p1[0] + p1[1]) + (p1[2] + p1[3])); });
    st8(v2u{asu(max4(own) * cs), asu(xred4(sm[0], sm[1], sm[2], sm[3], FAdd{}))}, c.rs,
        (lane >> 4) * 4096 + (lane & 15) * 8, O_ST + qb * 16384 + kb * 512 + w * 128);
    SB();
  }
  sfor<4>([&](auto oc) { const int so = voct(CV(oc)); dma_m0_h(mw + CV(oc) * 8192, vo, c.rs, so); dma_2nd_h(vo, c.rs, so); });
  waitv<8>();
  flag_set(c, F_P + qb * 32 + kb * 4 + w);
  STAMP(1, 6);
  v4u xv[5], gv[10];
  { const rsrc_t rx = mkrs(xin, 2048 * D * 2);
    const int ks0 = (9 * nb) >> 1;
    sfor<5>([&](auto uc) { ald16p<CV(uc) * 64>(xv[CV(uc)], (lane & 15) * (D * 2) + (lane >> 4) * 16, rx, qt * (16 * D * 2) + ks0 * 64); });
    sfor<10>([&](auto uc) { ald16<(CV(uc) >> 1) * 128 + (CV(uc) & 1) * 16>(gv[CV(uc)], (lane >> 4) * 32, c.rs, O_GATE + b * 4608 + ks0 * 128); }); }
  srange<4, 18>([&](auto oc) { const int so = voct(CV(oc)); dma_m0_h(mw + CV(oc) * 8192, vo, c.rs, so); dma_2nd_h(vo, c.rs, so); });
  flag_spin(c, F_P + qb * 32 + (lane & 31));
  STAMP(1, 7);
  f32x4 xf[10];
  sfor<5>([&](auto uc) { constexpr int u = CV(uc); pin(xv[u]);
    xf[2 * u] = f32x4{bflo(xv[u][0]), bfhi(xv[u][0]), bflo(xv[u][1]), bfhi(xv[u][1])}; xf[2 * u + 1] = f32x4{bflo(xv[u][2]), bfhi(xv[u][2]), bflo(xv[u][3]), bfhi(xv[u][3])}; });
  sfor<10>([&](auto uc) { pin(gv[CV(uc)]); });
  f32x4 tot[9];
  {
    constexpr Plan pl = P3::make();
    static_assert(pl.ok, "schedule");
    v2u sts[8];
    sfor<8>([&](auto ic) { ald8<CV(ic) * 128>(sts[CV(ic)], (lane >> 4) * 1024 + (lane & 15) * 8, c.rs, O_ST + qt * 4096); });
    const int sk = O_WKT + (b * 36 + ((9 * nb) >> 1)) * 5120 + w * 2048 - 36 * 8192, ab = O_P + qt * 32768;
    unsigned rd[3];
    rd[0] = c.lb + vo; rd[1] = rd[0] + 49152; rd[2] = rd[0] + 98304;
    v4u a[P3::AW], bf[2][9];
    f32x4 acc[9];
    float f[8];
    auto op = [&](auto oc) { constexpr int kind = pl.kind[CV(oc)], idx = pl.idx[CV(oc)];
      if constexpr (kind == OP_OCT) { const int so = idx < 36 ? voct(idx) : sk + idx * 8192; dma_m0_h(mw + ((8 * idx) % 144) * 1024, vo, c.rs, so); dma_2nd_h(vo, c.rs, so); }
      else ald16<(idx & 3) * 1024>(a[idx % P3::AW], vo, c.rs, ab + (((idx >> 2) + 2 * rot) & 7) * 4096); };
    auto rdb = [&](auto kc, auto jc) { constexpr int p = (CV(kc) * 9 + CV(jc)) % 144; dsr16<(p % 48) * 1024>(bf[CV(kc) & 1][CV(jc)], rd[p / 48]); };
    sfor<pl.pre>(op);
    bar();
    sfor<9>([&](auto jc) { rdb(IC(0), jc); });
    SB();
    sfor<32>([&](auto kc) {
      constexpr int k = CV(kc), no = pl.in1[k] - pl.in0[k];
      waitlv<pl.cnt[k]>();
      if constexpr (P3::isbar(k)) bar();
      sfor<9>([&](auto jc) { pin(bf[k & 1][CV(jc)]); });
      pin(a[k % P3::AW]);
      if constexpr (k == 1) {
        sfor<8>([&](auto ic) { pin(sts[CV(ic)]); });
        const float m0 = asf(sts[0][0]), m1 = asf(sts[4][0]), m = xmax16_32(fmaxf(m0, m1));
        const float e0 = __builtin_amdgcn_exp2f(m0 - m), e1 = __builtin_amdgcn_exp2f(m1 - m);
        const float s = __builtin_amdgcn_rcpf(xsum16_32(e0 * ((asf(sts[0][1]) + asf(sts[1][1])) + (asf(sts[2][1]) + asf(sts[3][1]))) + e1 * ((asf(sts[4][1]) + asf(sts[5][1])) + (asf(sts[6][1]) + asf(sts[7][1])))));
        for (int i = 0; i < 4; ++i) { f[2 * i] = __shfl(e0 * s, (lane & 15) + 16 * ((i + rot) & 3)); f[2 * i + 1] = __shfl(e1 * s, (lane & 15) + 16 * ((i + rot) & 3)); }
        waitl();
      }
      SB();
      sfor<9>([&](auto mc) {
        constexpr int m = CV(mc);
        if constexpr (k % 4 == 0 && k > 0) { if constexpr (k == 4) tot[m] = acc[m] * f[0]; else tot[m] += acc[m] * f[k / 4 - 1]; }
        acc[m] = mfma(bf[k & 1][m], a[k % P3::AW], k % 4 == 0 ? f32x4{0, 0, 0, 0} : acc[m]);
        SB();
        if constexpr (k + 1 < 32) srange<lo_of(m, 9, 7), lo_of(m + 1, 9, 7)>([&](auto rc) { rdb(IC(k + 1), rc); SB(); });
        srange<mid_of(m, no, 9), mid_of(m + 1, no, 9)>([&](auto qc) { op(IC(pl.in0[k] + CV(qc))); SB(); });
      });
    });
    sfor<9>([&](auto jc) { tot[CV(jc)] += acc[CV(jc)] * f[7]; });
  }
  STAMP(1, 8);
  waitv<0>(); bar();
  if (nb & 1) k3_epi<1>(c, tot, xf, gv, nb, qt); else k3_epi<0>(c, tot, xf, gv, nb, qt);
  const int qt4 = qb * 4 + (nb >> 1), nb4 = (nb & 1) * 4 + w;
  SB();
  v4u vp[9][3];
  flag_spin(c, F_X + 36 + b * 18 + (lane < 18 ? lane : 0));
  flag_set(c, F_S + qt * 8 + nb);
  STAMP(1, 9);
  sfor<27>([&](auto ic) { constexpr int i = CV(ic); ald16<(i & 3) * 1024>(vp[i / 3][i % 3], vo, c.rs, O_VPT + (b * 72 + 9 * nb4) * 3072 + (i >> 2) * 4096); });
  LAS float* ured = (LAS float*)(c.lds + LDS_MISC + 2048);
  { const int tid = w * 64 + lane;
    if (tid < 240) { const int g = tid / 80, m = tid % 80; float u = 0.f;
      for (int i = 0; i < 6; ++i) u += asf(__builtin_amdgcn_raw_buffer_load_b32(c.rs, (g * 6 + i) * 320 + m * 4, O_U + b * 5760, SC1));
      ured[tid] = u; } }
  if (blockIdx.x == 0 && w == 3) {
    sfor<4>([&](auto ic) { flag_spin(c, F_T + CV(ic) * 64 + lane); });
    if (lane == 0) __builtin_amdgcn_raw_buffer_store_b64(c.tag(), c.rs, 0, O_EP, SC1);
  }
  flag_spin(c, F_S + qt4 * 8 + (lane & 7));
  STAMP(1, 10);
  {
    const int r = lane & 15, q = lane >> 4;
    v4u sp[2][5], x2[5];
    float lp0, lp1;
    sfor<10>([&](auto ic) { constexpr int i = CV(ic); ald16<(i & 3) * 1024>(sp[i / 5][i % 5], vo, c.rs, O_SCP + (qt4 * 8 + 2 * w) * 5120 + (i >> 2) * 4096); });
    asm volatile("buffer_load_dword %0, %2, %3, %4 offen sc1\n\tbuffer_load_dword %1, %2, %3, %4 offen offset:64 sc1" : "=&v"(lp0), "=&v"(lp1) : "v"(q * 128 + r * 4), "s"(c.rs), "s"(O_LNP + qt4 * 512));
    sfor<5>([&](auto uc) { constexpr int u = CV(uc); ald16<(u & 3) * 1024>(x2[u], vo, c.rs, O_X2 + (qt4 * 36 + ((9 * nb4) >> 1)) * 1024 + (u >> 2) * 4096); });
    waitv<5>();
    sfor<10>([&](auto ic) { pin(sp[CV(ic) / 5][CV(ic) % 5]); });
    pin(lp0); pin(lp1);
    LAS f32x4* red = (LAS f32x4*)(c.lds + 32768);
    sfor<5>([&](auto tc) { constexpr int t = CV(tc); red[(w * 5 + t) * 64 + lane] = __builtin_bit_cast(f32x4, sp[0][t]) + __builtin_bit_cast(f32x4, sp[1][t]); });
    const float sq = xsum16_32(lp0 + lp1) * (1.f / D);
    lbar();
    f32x4 s[5];
    float mx = -3.0e38f;
    sfor<5>([&](auto tc) { constexpr int t = CV(tc); s[t] = (red[t * 64 + lane] + red[(5 + t) * 64 + lane]) + (red[(10 + t) * 64 + lane] + red[(15 + t) * 64 + lane]); });
    const float mean = xsum16_32(q == 3 ? s[4][1] : 0.f) * (1.f / D);
    const float cc = 0.029462782549439476f * 1.4426950408889634f * __builtin_amdgcn_rsqf(fmaxf(sq - mean * mean, 0.f) + 1e-5f);
    sfor<5>([&](auto tc) { constexpr int t = CV(tc);
      const LAS f32x4* u4 = (const LAS f32x4*)(ured + t * 16 + q * 4);
      s[t] = (s[t] - ((u4[0] + u4[20]) + u4[40]) * mean) * cc;
      if constexpr (t == 4) for (int e = 1; e < 4; ++e) s[t][e] = q == 3 ? -3.0e38f : s[t][e];
      mx = max3(max3(mx, s[t][0], s[t][1]), s[t][2], s[t][3]); });
    mx = xmax16_32(mx);
    float sum = 0.f;
    sfor<5>([&](auto tc) { constexpr int t = CV(tc); for (int e = 0; e < 4; ++e) { s[t][e] = __builtin_amdgcn_exp2f(s[t][e] - mx); sum += s[t][e]; } });
    sum = __builtin_amdgcn_rcpf(xsum16_32(sum));
    v4u pc[3];
    pc[0] = pack8(s[0] * sum, s[1] * sum); pc[1] = pack8(s[2] * sum, s[3] * sum); pc[2] = pack8(s[4] * sum, f32x4{0, 0, 0, 0});
    waitv<0>();
    sfor<27>([&](auto ic) { pin(vp[CV(ic) / 3][CV(ic) % 3]); });
    sfor<5>([&](auto uc) { pin(x2[CV(uc)]); });
    const rsrc_t ro = mkrs(out, 2048 * D * 2);
    if (w & 1) k4_out<1>(vp, pc, x2, ro, nb4, qt4, lane); else k4_out<0>(vp, pc, x2, ro, nb4, qt4, lane);
  }
}
}

extern "C" void kernel_launch(void* const* d_in, const int* in_sizes, int n_in, void* d_out, int out_size, void* d_ws, size_t ws_size, hipStream_t stream) {
  (void)hipFuncSetAttribute((const void*)fused, hipFuncAttributeMaxDynamicSharedMemorySize, LDS_BYTES);
  fused<<<256, 256, LDS_BYTES, stream>>>((char*)d_ws + (WSEND ? ((ws_size - WS_BYTES) & ~(size_t)0xfffff) : 0), (const us*)d_in[0], (const us*)d_in[1], (const us*)d_in[2], (const us*)d_in[3], (const us*)d_in[4],
                                         (const us*)d_in[5], (const us*)d_in[6], (const us*)d_in[7], (const us*)d_in[8], (us*)d_out);
}
```

```cpp
#define OPOL "sc1"
#define OAUX 16
#include <hip/hip_runtime.h>
#include <utility>
#include <stdint.h>

#define NTPOL 1
#define WSEND 0
#define STAG 3
#define PREC 1
#define PRET 0
#define WNT 0
#define FS 64
#define SLP 2
#define MAP48 1
namespace {
typedef unsigned v4u __attribute__((ext_vector_type(4)));
typedef unsigned v2u __attribute__((ext_vector_type(2)));
typedef float f32x4 __attribute__((ext_vector_type(4)));
typedef float f32x2 __attribute__((ext_vector_type(2)));
typedef short bf16x8 __attribute__((ext_vector_type(8)));
typedef __bf16 bf16x2_t __attribute__((ext_vector_type(2)));
typedef __amdgpu_buffer_rsrc_t rsrc_t;
typedef unsigned short us;
#define LAS __attribute__((address_space(3)))
#define DEV __device__ __forceinline__
#define SB() __builtin_amdgcn_sched_barrier(0)
#define IC(x) std::integral_constant<int, (x)>{}
#define CV(x) decltype(x)::value

template <int B, int... I, class F> DEV void sfor_impl(std::integer_sequence<int, I...>, F&& f) { (f(std::integral_constant<int, B + I>{}), ...); }
template <int N, class F> DEV void sfor(F&& f) { sfor_impl<0>(std::make_integer_sequence<int, N>{}, f); }
template <int B, int E, class F> DEV void srange(F&& f) { sfor_impl<B>(std::make_integer_sequence<int, (E > B ? E - B : 0)>{}, f); }

constexpr int D = 1152, KS = 36;
constexpr unsigned O_EP = 0;
constexpr unsigned O_FL = 256;
constexpr int F_MU = 0, F_G = 192, F_H = 256, F_W = 384, F_KVC = 576, F_X = 704, F_T = 1024, F_V = 1280, F_P = 1536, F_S = 2560;
constexpr unsigned O_GATE = 524288;
constexpr unsigned O_MODP = O_GATE + 16384;
constexpr unsigned O_H = O_MODP + 81920;
constexpr unsigned O_WT = O_H + 64 * 72 * 1024;
constexpr unsigned O_T = O_WT + (16 * 324 + 8) * 1024;
constexpr unsigned O_VT = O_T + 32 * 144 * 1024;
constexpr unsigned O_P = O_VT + 16 * 288 * 1024;
constexpr unsigned O_ST = O_P + 128 * 32 * 1024;
constexpr unsigned O_KC = O_ST + 128 * 4096;
constexpr unsigned O_VC = O_KC + 360 * 1024;
constexpr unsigned O_WKT = O_VC + 360 * 1024;
constexpr unsigned O_VPT = O_WKT + 368 * 1024;
constexpr unsigned O_X2 = O_VPT + 432 * 1024;
constexpr unsigned O_SCP = O_X2 + 128 * 36 * 1024;
constexpr unsigned O_LNP = O_SCP + 128 * 40 * 1024;
constexpr unsigned O_U = O_LNP + 128 * 1024;
constexpr unsigned WS_BYTES = O_U + 2 * 18 * 80 * 4;
constexpr int LDS_MISC = 147456, LDS_BYTES = 163840;
constexpr int SC1 = 16;

#define STAMP(k, s)

DEV f32x4 mfma(v4u a, v4u b, f32x4 c) {
  return __builtin_amdgcn_mfma_f32_16x16x32_bf16(__builtin_bit_cast(bf16x8, a), __builtin_bit_cast(bf16x8, b), c, 0, 0, 0);
}
DEV unsigned pk2(float a, float b) { f32x2 v = {a, b}; return __builtin_bit_cast(unsigned, __builtin_convertvector(v, bf16x2_t)); }
DEV float asf(unsigned u) { return __builtin_bit_cast(float, u); }
DEV unsigned asu(float f) { return __builtin_bit_cast(unsigned, f); }
DEV float bflo(unsigned u) { return __builtin_bit_cast(float, u << 16); }
DEV float bfhi(unsigned u) { return __builtin_bit_cast(float, u & 0xffff0000u); }
DEV v4u pack8(f32x4 a, f32x4 b) { return v4u{pk2(a[0], a[1]), pk2(a[2], a[3]), pk2(b[0], b[1]), pk2(b[2], b[3])}; }
DEV v2u pack4(f32x4 a) { return v2u{pk2(a[0], a[1]), pk2(a[2], a[3])}; }
DEV rsrc_t mkrs(const void* p, unsigned bytes) { return __builtin_amdgcn_make_buffer_rsrc((void*)p, 0, bytes, 0x00020000); }
template <int IMM> DEV void set_m0(unsigned base) { asm volatile("s_add_u32 m0, %0, %1" ::"s"(base), "i"(IMM) : "scc"); }
template <int POL, int OFF> DEV void dma(int vo, rsrc_t rs, int so) {
  if constexpr (POL == 0) asm volatile("buffer_load_dwordx4 %0, %1, %2 offen offset:%3 lds" ::"v"(vo), "s"(rs), "s"(so), "i"(OFF) : "memory");
  else if constexpr (POL == 1) asm volatile("buffer_load_dwordx4 %0, %1, %2 offen offset:%3 nt lds" ::"v"(vo), "s"(rs), "s"(so), "i"(OFF) : "memory");
  else asm volatile("buffer_load_dwordx4 %0, %1, %2 offen offset:%3 sc1 lds" ::"v"(vo), "s"(rs), "s"(so), "i"(OFF) : "memory");
}
DEV void nop1() { asm volatile("s_nop 0"); }
template <int OFF> DEV void ald16(v4u& d, int vo, rsrc_t rs, int so) {
  asm volatile("buffer_load_dwordx4 %0, %1, %2, %3 offen offset:%4 sc1" : "=v"(d) : "v"(vo), "s"(rs), "s"(so), "i"(OFF));
}
template <int OFF> DEV void ald16p(v4u& d, int vo, rsrc_t rs, int so) {
  asm volatile("buffer_load_dwordx4 %0, %1, %2, %3 offen offset:%4" : "=v"(d) : "v"(vo), "s"(rs), "s"(so), "i"(OFF));
}
template <int OFF> DEV void ald16a(v4u& d, int vo, rsrc_t rs, int so) {
  asm volatile("buffer_load_dwordx4 %0, %1, %2, %3 offen offset:%4 sc1" : "=a"(d) : "v"(vo), "s"(rs), "s"(so), "i"(OFF));
}
template <int OFF> DEV void ald8(v2u& d, int vo, rsrc_t rs, int so) {
  asm volatile("buffer_load_dwordx2 %0, %1, %2, %3 offen offset:%4 sc1" : "=v"(d) : "v"(vo), "s"(rs), "s"(so), "i"(OFF));
}
template <int OFF> DEV void dsr16(v4u& d, unsigned addr) { asm volatile("ds_read_b128 %0, %1 offset:%2" : "=v"(d) : "v"(addr), "i"(OFF)); }
template <int OFF> DEV void dsrtr(v2u& d, unsigned addr) { asm volatile("ds_read_b64_tr_b16 %0, %1 offset:%2" : "=v"(d) : "v"(addr), "i"(OFF)); }
template <int N> DEV void waitv() { asm volatile("s_waitcnt vmcnt(%0)" ::"i"(N) : "memory"); }
template <int N> DEV void waitlv() { asm volatile("s_waitcnt vmcnt(%0) lgkmcnt(0)" ::"i"(N) : "memory"); }
template <int N, int L> DEV void waitvl() { asm volatile("s_waitcnt vmcnt(%0) lgkmcnt(%1)" ::"i"(N), "i"(L) : "memory"); }
template <int L> DEV void waitln() { asm volatile("s_waitcnt lgkmcnt(%0)" ::"i"(L) : "memory"); }
DEV void waitl() { asm volatile("s_waitcnt lgkmcnt(0)" ::: "memory"); }
DEV void bar() { asm volatile("s_barrier" ::: "memory"); }
DEV void lbar() { asm volatile("s_waitcnt lgkmcnt(0)\n\ts_barrier" ::: "memory"); }
DEV void pin(v4u& x) { asm volatile("" : "+v"(x)); }
DEV void pin(v2u& x) { asm volatile("" : "+v"(x)); }
DEV void pins(int& x) { asm volatile("" : "+s"(x)); }
DEV void pin(int& x) { asm volatile("" : "+v"(x)); }
DEV void pin(float& x) { asm volatile("" : "+v"(x)); }
DEV void pin(f32x4& x) { asm volatile("" : "+v"(x)); }
DEV void pin(f32x2& x) { asm volatile("" : "+v"(x)); }
DEV void st16(v4u v, rsrc_t rs, int vo, int so) { asm volatile("buffer_store_dwordx4 %0, %1, %2, %3 offen sc1\n\ts_nop 1" ::"v"(v), "v"(vo), "s"(rs), "s"(so) : "memory"); }
template <int OFF> DEV void st16o(v4u v, rsrc_t rs, int vo) { asm volatile("buffer_store_dwordx4 %0, %1, %2, 0 offen offset:%3 " OPOL "\n\ts_nop 1" ::"v"(v), "v"(vo), "s"(rs), "i"(OFF) : "memory"); }
DEV float max3(float a, float b, float c) { float r; asm("v_max3_f32 %0, %1, %2, %3" : "=v"(r) : "v"(a), "v"(b), "v"(c)); return r; }
DEV float max4(f32x4 a) { return max3(max3(a[0], a[1], a[2]), a[3], a[3]); }
DEV void st8(v2u v, rsrc_t rs, int vo, int so) { __builtin_amdgcn_raw_buffer_store_b64(v, rs, vo, so, SC1); }
DEV v4u ld16(rsrc_t rs, int vo, int so) { return __builtin_amdgcn_raw_buffer_load_b128(rs, vo, so, 0); }
DEV v4u ld16h(rsrc_t rs, int vo, int so) { return __builtin_amdgcn_raw_buffer_load_b128(rs, vo, so, SC1); }
struct FMax { DEV float operator()(float a, float b) const { return max3(a, b, b); } };
struct FAdd { DEV float operator()(float a, float b) const { return a + b; } };
template <class Op> DEV float sw32(float a, float b, Op op) { asm("s_nop 1\n\tv_permlane32_swap_b32 %0, %1" : "+v"(a), "+v"(b)); return op(a, b); }
template <class Op> DEV float sw16(float a, float b, Op op) { asm("s_nop 1\n\tv_permlane16_swap_b32 %0, %1" : "+v"(a), "+v"(b)); return op(a, b); }
DEV float xmax16_32(float v) { v = sw16(v, v, FMax{}); return sw32(v, v, FMax{}); }
DEV float xsum16_32(float v) { v = sw16(v, v, FAdd{}); return sw32(v, v, FAdd{}); }
template <class Op> DEV float xred4(float v0, float v1, float v2, float v3, Op op) { return sw16(sw32(v0, v2, op), sw32(v1, v3, op), op); }
DEV void rows4(float v, float& r0, float& r1, float& r2, float& r3) {
  float a = v, b = v;
  asm("s_nop 1\n\tv_permlane16_swap_b32 %0, %1" : "+v"(a), "+v"(b));
  r0 = a; r2 = a; r1 = b; r3 = b;
  asm("s_nop 1\n\tv_permlane32_swap_b32 %0, %1" : "+v"(r0), "+v"(r2));
  asm("s_nop 1\n\tv_permlane32_swap_b32 %0, %1" : "+v"(r1), "+v"(r3));
}
DEV void touch(unsigned lb, int vo, rsrc_t rs, int so) {
  asm volatile("s_mov_b32 m0, %0\n\ts_nop 0\n\tbuffer_load_dword %1, %2, %3 offen lds" ::"s"(lb + LDS_BYTES - 256), "v"(vo), "s"(rs), "s"(so) : "memory");
}
template <bool HA, bool H_IS_A, bool FIRST> DEV void mma_h(f32x4& acc, const v4u& h, const v4u& o) {
#define MMA_(CH, X, Y) \
  if constexpr (FIRST) asm volatile("v_mfma_f32_16x16x32_bf16 %0, " X ", " Y ", 0" : "=&v"(acc) : CH(h), "v"(o)); \
  else asm volatile("v_mfma_f32_16x16x32_bf16 %0, " X ", " Y ", %0" : "+v"(acc) : CH(h), "v"(o));
  if constexpr (HA) { if constexpr (H_IS_A) { MMA_("a", "%1", "%2") } else { MMA_("a", "%2", "%1") } }
  else { if constexpr (H_IS_A) { MMA_("v", "%1", "%2") } else { MMA_("v", "%2", "%1") } }
#undef MMA_
}
#define IPF 1
template <int ID> DEV void istub() { if constexpr (IPF) asm volatile("s_branch .Lsk%c0\n.Lst%c0:\n\ts_setpc_b64 s[98:99]\n.Lsk%c0:" ::"i"(ID)); }
template <int ID> DEV void iprobe1() { asm volatile("s_call_b64 s[98:99], .Lst%c0" ::"i"(ID) : "s98", "s99"); }
template <int B, int E> DEV void iprobe(int w) { if constexpr (IPF) srange<B, E>([&](auto ic) { if (w == (CV(ic) & 3)) iprobe1<CV(ic)>(); }); }
DEV void mfma_settle() { asm volatile("s_nop 15" ::: "memory"); SB(); }

struct Ctx {
  char* ws; rsrc_t rs; unsigned lb; LAS char* lds; int lane, w; unsigned t0, t1;
  DEV void make_tag() {
    const v2u ep = *(volatile LAS v2u*)(lds + LDS_BYTES - 512 - w * 256);
    const unsigned long long t = (((unsigned long long)(unsigned)__builtin_amdgcn_readfirstlane(ep[1]) << 32) | (unsigned)__builtin_amdgcn_readfirstlane(ep[0])) + 0x9E3779B97F4A7C15ull;
    t0 = (unsigned)t; t1 = (unsigned)(t >> 32);
  }
  DEV v2u tag() const { return v2u{t0, t1}; }
};
DEV void flag_set(const Ctx& c, int f) { if (c.lane == 0) __builtin_amdgcn_raw_buffer_store_b64(c.tag(), c.rs, 0, O_FL + f * FS, SC1); }
DEV bool flag_ok(const Ctx& c, v2u v) { return __all(v[0] == c.t0 && v[1] == c.t1); }
DEV void flag_spin(const Ctx& c, int f) {
#pragma unroll 1
  for (int i = 0; i < 20000; ++i) {
    v2u v;
    asm volatile("buffer_load_dwordx2 %0, %1, %2, 0 offen offset:%3 sc1\n\ts_waitcnt vmcnt(0)" : "=&v"(v) : "v"(f * FS), "s"(c.rs), "i"(O_FL) : "memory");
    if (flag_ok(c, v)) return;
    __builtin_amdgcn_s_sleep(SLP);
  }
}
DEV void publish(const Ctx& c, int f) { waitv<0>(); bar(); if (c.w == 0) flag_set(c, f); }
DEV void publish_first(Ctx& c, int f) { waitv<0>(); c.make_tag(); bar(); if (c.w == 0) flag_set(c, f); }

template <int NM, int NR, int NO, int SPAN> struct Gaps {
  int o0[NM + 1] = {}, r0[NM + 1] = {};
  constexpr Gaps() {
    int fr[NM] = {}, nf = 0, q = 0;
    for (int m = 0; m < NM; ++m) { o0[m] = q; while (q < NO && ((2 * q + 1) * NM) / (2 * NO + !NO) == m) ++q; if (q == o0[m] && m < SPAN) fr[nf++] = m; }
    o0[NM] = NO;
    if (nf == 0) for (; nf < NM; ++nf) fr[nf] = nf;
    int r = 0;
    for (int m = 0; m < NM; ++m) { r0[m] = r; while (r < NR && fr[(r * nf) / (NR + !NR)] == m) ++r; }
    r0[NM] = NR;
  }
};
template <int NM, int NR, int NO, int SPAN = NM, class Mm, class Rd, class Prep, class Op> DEV void kstep(Mm&& mm, Rd&& rd, Prep&& prep, Op&& op) {
  constexpr Gaps<NM, NR, NO, SPAN> g{};
  srange<g.o0[0], g.o0[1]>([&](auto qc) { if constexpr (CV(qc) == g.o0[0]) prep(qc); });
  SB();
  sfor<NM>([&](auto mc) { constexpr int m = CV(mc);
    mm(mc); SB();
    srange<g.o0[m], g.o0[m + 1]>([&](auto qc) { if constexpr (CV(qc) > g.o0[m]) { prep(qc); nop1(); } op(qc); SB(); });
    srange<g.r0[m], g.r0[m + 1]>([&](auto rc) { rd(rc); SB(); });
    if constexpr (m + 1 < NM && g.o0[m + 2 > NM ? NM : m + 2] > g.o0[m + 1]) { prep(IC(g.o0[m + 1])); SB(); }
  });
}
template <int ROW, int SID, int SPLIT, int RA, int DS, int NK, int NP, int NR, int NM, class Hook, class Prep, class Issue, class Rd, class Pin, class Mm>
DEV void raw_loop(const Ctx& c, Hook&& hook, Prep&& prep, Issue&& issue, Rd&& rd, Pin&& pinf, Mm&& mm) {
  constexpr int NS = KS / NK;
  STAMP(ROW, 1);
  istub<SID>();
  if constexpr (SPLIT) {
    sfor<2>([&](auto sc) { sfor<SPLIT>([&](auto pc) { prep(sc, pc); nop1(); issue(sc, pc); }); });
    hook(IC(-1));
    sfor<2>([&](auto sc) { srange<SPLIT, NP>([&](auto pc) { prep(sc, pc); nop1(); issue(sc, pc); }); });
  } else {
    sfor<2>([&](auto sc) { sfor<NP>([&](auto pc) { prep(sc, pc); nop1(); issue(sc, pc); }); });
    hook(IC(-1));
  }
  if constexpr (DS == 3) sfor<NP>([&](auto pc) { prep(IC(2), pc); nop1(); issue(IC(2), pc); });
  SB();
  STAMP(ROW, 2);
  waitv<(DS - 1) * NP - SPLIT>(); bar();
  STAMP(ROW, 3);
  hook(IC(-2));
  sfor<RA * NR>([&](auto rc) { rd(IC(CV(rc) / NR), IC(CV(rc) % NR)); });
  SB();
  sfor<KS>([&](auto kc) {
    constexpr int k = CV(kc), s = k / NK, kk = k % NK, lg = RA == 2 && k + 1 < KS ? NR : 0;
    constexpr int p0 = (kk * NP) / NK, p1 = ((kk + 1) * NP) / NK, np = (s + DS < NS) ? p1 - p0 : 0;
    if constexpr (kk == NK - RA && s + 1 < NS) { waitvl<(s + DS < NS ? p0 : 0) + (DS == 3 && s + 2 < NS ? NP : 0), lg>(); bar(); }
    else waitln<lg>();
    if constexpr (k % 8 == 4) istub<SID + 1 + k / 8>();
    pinf(kc);
    hook(kc);
    SB();
    kstep<NM, (k + RA < KS ? NR : 0), np, (RA == 1 ? NM - NM / 4 : NM)>([&](auto mc) { mm(kc, mc); }, [&](auto rc) { rd(IC(k + RA), rc); },
      [&](auto qc) { prep(IC(s + DS), IC(p0 + CV(qc))); }, [&](auto qc) { issue(IC(s + DS), IC(p0 + CV(qc))); });
    if constexpr (k == 11) STAMP(ROW, 4);
    if constexpr (k == 23) STAMP(ROW, 5);
  });
  istub<SID + 5>();
  STAMP(ROW, 6);
}

DEV unsigned kc_rd(unsigned base, int lane, int h) { const int r = lane & 15, q = lane >> 4; return base + r * 128 + (((4 * h + q) ^ (r & 7)) << 4); }
template <bool PI> DEV int kc_src(int lane, int stride) { const int rr = lane >> 3; return (PI ? 8 * (rr >> 2) + (rr & 3) : rr) * stride + (((lane & 7) ^ rr) << 4); }
DEV int tr64_src(int lane, int par, int stride) { const int rr = lane >> 3; return rr * stride + (((lane & 7) ^ ((((rr >> 1) & 1) | ((par & 1) << 1)) << 1)) << 4); }
DEV unsigned tr64_rd(unsigned base, int lane, int w) {
  const int gg = lane >> 4, qq = (lane & 15) >> 2, pp = lane & 3;
  return base + (8 * gg + qq) * 128 + (((2 * w + (pp >> 1)) ^ (((qq >> 1) | ((gg & 1) << 1)) << 1)) << 4) + 8 * (pp & 1);
}
DEV int pi_inv_off(int t, int lane, int blk0, int blk1) {
  const int xx = 16 * (t & 1) + (lane & 15);
  return (((xx >> 2) & 1) ? blk1 : blk0) + (((lane >> 4) * 16 + 4 * (xx >> 3) + (xx & 3)) << 4);
}
DEV v4u cat(v2u a, v2u b) { return v4u{a[0], a[1], b[0], b[1]}; }
DEV v4u silu8(v4u v) {
  auto f = [](float x) { return x * __builtin_amdgcn_rcpf(1.f + __builtin_amdgcn_exp2f(x * -1.4426950408889634f)); };
  v4u o;
  for (int e = 0; e < 4; ++e) o[e] = pk2(f(bflo(v[e])), f(bfhi(v[e])));
  return o;
}

template <int TRB> DEV void w_tile(Ctx& c, const us* wqkv, const us* wo, const us* cin, const us* wmod, int ti, int tj, int pi, int ni, int pj, int nj, int mu) {
  const int lane = c.lane, w = c.w, wr = w >> 1, wc = w & 1;
  const rsrc_t ra = mkrs(wqkv, D * 3456 * 2), rb = TRB ? mkrs(wo, D * D * 2) : ra;
  const unsigned lb = c.lb;
  int va[4], vb[4];
  sfor<4>([&](auto uc) { constexpr int u = CV(uc);
    va[u] = kc_src<true>(lane, 6912) + (16 * (u & 1) + 4 * (u >> 1)) * 6912 - u * 1024;
    vb[u] = TRB ? (4 * u + (lane >> 4)) * 2304 + (((lane & 15) ^ (((lane >> 4) << 2) | u)) << 4) - u * 1024 : kc_src<false>(lane, 6912) + 8 * u * 6912 - u * 1024; });
  const int sa = (ti * 128 + 32 * w) * 6912 + (TRB ? 4608 : 0), sb = TRB ? tj * 256 + 16 * w * 2304 : (tj * 128 + 32 * w) * 6912 + 2304;
  unsigned ra_[2][2], rb_[2][8];
  sfor<2>([&](auto gc) { constexpr int g = CV(gc);
    sfor<2>([&](auto hc) { ra_[g][CV(hc)] = kc_rd(lb + g * 65536 + wr * 8192, lane, CV(hc)); });
    if constexpr (TRB) {
      const int gg = lane >> 4, qq = (lane & 15) >> 2, pp = lane & 3;
      sfor<8>([&](auto ec) { constexpr int cc = CV(ec) >> 1, t = CV(ec) & 1;
        const int row = 8 * gg + 4 * t + qq, ch = 2 * (4 * wc + cc) + (pp >> 1);
        rb_[g][CV(ec)] = lb + g * 65536 + 16384 + 256 * row + 16 * (ch ^ ((qq << 2) | ((2 * gg + t) & 3))) + 8 * (pp & 1); });
    } else sfor<2>([&](auto hc) { rb_[g][CV(hc)] = kc_rd(lb + g * 65536 + 16384 + wc * 8192, lane, CV(hc)); }); });
  const int so0 = STAG == 5 ? ((pi + pj) * 18 / 5) % 18 : STAG == 0 ? 0 : ((pi + pj) % STAG) * (18 / (STAG ? STAG : 1));
  auto stg = [&](int st) { const int t = st + so0; return t >= 18 ? t - 18 : t; };
  int eo[4][2];
  const int strip = mu >> 2, kq = mu & 3, dead = mu < 144 ? 0 : 0x7ff00000;
  v4u cv;
  ald16p<0>(cv, (lane < 36 && w < 2 ? lane * 16 : 0x7ff00000) | dead, mkrs(cin, 2 * D * 2), (w & 1) * 2304 + kq * 576);
  { const rsrc_t rw = mkrs(wmod, D * 6912 * 2);
    const int v64 = tr64_src(lane, w, 13824) | dead, s64 = strip * 128 + (kq * 288 + 8 * w) * 13824;
    sfor<9>([&](auto pc) { set_m0<98304 + CV(pc) * 4096>(lb + w * 1024); nop1(); dma<1, 0>(v64, rw, s64 + CV(pc) * 32 * 13824); }); }
  sfor<4>([&](auto jc) { constexpr int j = CV(jc);
    const int xx = 16 * (j & 1) + (lane & 15), T = tj * 8 + wc * 4 + (j & 2) + ((xx >> 2) & 1), lo = TRB ? 60 : 48;
    const bool hi = T >= lo;
    const int n = hi ? (TRB ? 3 : 6) : (TRB ? 5 : 4), U = hi ? T - lo : T, sl = (U * (hi ? (TRB ? 86 : 43) : (TRB ? 52 : 64))) >> 8;
    eo[j][0] = ((sl + (hi ? 12 : 0)) * 324 + (TRB ? 324 - 36 * n : 0) + (ti * 4 + wr * 2) * n + U - sl * n) * 1024 + (((lane >> 4) * 16 + 4 * (xx >> 3) + (xx & 3)) << 4);
    eo[j][1] = eo[j][0] + n * 1024; pin(eo[j][0]); pin(eo[j][1]); });
  iprobe<TRB * 8, TRB * 8 + 6>(w);
  { waitv<9>();
    pin(cv);
    LAS v4u* sc = (LAS v4u*)(c.lds + LDS_MISC);
    if (w < 2) { if (lane < 36) sc[w * 36 + lane] = silu8(cv); } else if (lane == 0) sc[72] = v4u{0, 0, 0, 0};
    c.make_tag();
    waitlv<0>(); bar();
    const int r = lane & 15, q = lane >> 4;
    const unsigned cb = lb + LDS_MISC + (r < 2 ? r * 576 + q * 16 : 1152), cs = r < 2 ? 64 : 0, ra2 = tr64_rd(lb + 98304, lane, w);
    v2u mt[9][2]; v4u cf[9];
    sfor<9>([&](auto ic) { constexpr int i = CV(ic); dsrtr<i * 4096>(mt[i][0], ra2); dsrtr<i * 4096 + 512>(mt[i][1], ra2); dsr16<0>(cf[i], cb + i * cs); });
    waitl();
    f32x4 m = {0, 0, 0, 0};
    sfor<9>([&](auto ic) { constexpr int i = CV(ic); pin(mt[i][0]); pin(mt[i][1]); pin(cf[i]); m = mfma(cat(mt[i][0], mt[i][1]), cf[i], m); });
    pin(m); mfma_settle();
    st16(__builtin_bit_cast(v4u, m), c.rs, (r < 2 ? r * 36864 + q * 16 : 0x7ff00000) | dead, O_MODP + kq * 9216 + strip * 256 + w * 64);
  }
  v4u fa[3][4], fb[3][4];
  v2u ft[3][8];
  f32x4 acc[4][4];
  const unsigned mw = lb + w * 4096;
  int so;
  raw_loop<0, TRB * 8, 0, 1, 3, 2, 8, TRB ? 12 : 8, 16>(c,
    [&](auto kc) { if constexpr (CV(kc) == -1) { waitv<16>(); bar(); if (w == 0 && mu < 144) flag_set(c, F_MU + mu); } },
    [&](auto sc, auto pc) { constexpr int s = CV(sc), p = CV(pc);
      if constexpr ((p & 3) == 0) { so = p == 0 ? sa + stg(s) * 128 : sb + stg(s) * (TRB ? 64 * 2304 : 128); pins(so); set_m0<(s & 3) * 32768 + (p >> 2) * 16384>(mw); } },
    [&](auto, auto pc) { constexpr int p = CV(pc); if constexpr (p < 4) dma<0, p * 1024>(va[p], ra, so); else dma<0, (p & 3) * 1024>(vb[p & 3], rb, so); },
    [&](auto kc, auto rc) { constexpr int k = CV(kc), r = CV(rc), s = (k >> 1) & 3, h = k & 1;
      if constexpr (r < 4) dsr16<(s & 1) * 32768 + r * 2048>(fa[k % 3][r], ra_[s >> 1][h]);
      else if constexpr (TRB) dsrtr<(s & 1) * 32768 + h * 8192>(ft[k % 3][r - 4], rb_[s >> 1][r - 4]);
      else dsr16<(s & 1) * 32768 + (r - 4) * 2048>(fb[k % 3][r - 4], rb_[s >> 1][h]); },
    [&](auto kc) { constexpr int k = CV(kc);
      sfor<4>([&](auto ic) { pin(fa[k % 3][CV(ic)]); });
      if constexpr (TRB) sfor<4>([&](auto ic) { constexpr int i = CV(ic); pin(ft[k % 3][2 * i]); pin(ft[k % 3][2 * i + 1]); fb[k % 3][i] = cat(ft[k % 3][2 * i], ft[k % 3][2 * i + 1]); });
      else sfor<4>([&](auto ic) { pin(fb[k % 3][CV(ic)]); }); },
    [&](auto kc, auto mc) { constexpr int k = CV(kc), i = CV(mc) & 3, j = CV(mc) >> 2;
      acc[i][j] = mfma(fa[k % 3][i], fb[k % 3][j], k == 0 ? f32x4{0, 0, 0, 0} : acc[i][j]); });
  sfor<4>([&](auto jc) { constexpr int j = CV(jc);
    sfor<2>([&](auto vc) { constexpr int v = CV(vc); st16(pack8(acc[2 * v][j], acc[2 * v + 1][j]), c.rs, eo[j][v], O_WT); }); });
  STAMP(0, 7);
  publish(c, F_W + (TRB * 9 + tj) * 9 + ti);
  STAMP(0, 8);
}

template <int KIND, class Hook> DEV void small_tile(Ctx& c, const us* wgt, const us* y, int b, int t, int rot, Hook&& hook) {
  constexpr bool TR = KIND != 0;
  constexpr int LD = KIND == 2 ? 4608 : D * 2;
  const int lane = c.lane, w = c.w;
  const rsrc_t r64 = mkrs(wgt, D * LD), r80 = KIND == 2 ? mkrs(y, 154 * D * 2) : c.rs;
  int v64[4], v80[5];
  sfor<4>([&](auto uc) { constexpr int u = CV(uc);
    v64[u] = TR ? tr64_src(lane, u, LD) + 8 * u * LD - u * 1024 : kc_src<true>(lane, LD) + (16 * (u & 1) + 4 * (u >> 1)) * LD - u * 1024; });
  sfor<5>([&](auto uc) { constexpr int u = CV(uc);
    v80[u] = KIND == 2 ? kc_src<false>(lane, D * 2) + 8 * u * (D * 2) - (u & 3) * 1024 : lane * 16 + (u >> 2) * 4096; });
  if (KIND == 2 && (lane >> 3) >= 5 && (w & 1)) v80[4] = 0x7ff00000;
  const int s64 = TR ? t * 128 + 32 * w * LD : (t * 64 + 32 * (w & 1)) * LD + (w >> 1) * 128;
  const int s80 = KIND == 2 ? (b * 77 + (w & 1) * 40) * (D * 2) + (w >> 1) * 128 : (KIND ? O_VC : O_KC) + b * (180 * 1024) + w * 5120;
  auto stg = [&](int st) { const int u = st + rot; return u >= 9 ? u - 9 : u; };
  unsigned r64a[3][2], r80a[3][2];
  sfor<3>([&](auto gc) { constexpr int g = CV(gc); const unsigned base = c.lb + g * 49152;
    if constexpr (TR) r64a[g][0] = tr64_rd(base, lane, w); else sfor<2>([&](auto hc) { r64a[g][CV(hc)] = kc_rd(base + w * 2048, lane, CV(hc)); });
    if constexpr (KIND == 2) sfor<2>([&](auto hc) { r80a[g][CV(hc)] = kc_rd(base, lane, CV(hc)); }); else r80a[g][0] = base + lane * 16; });
  v4u f64[3], f80[3][5];
  v2u ft[3][2];
  f32x4 acc[5];
  const unsigned m64 = c.lb + w * 4096, m80 = c.lb + 16384 + w * 5120;
  int so;
  raw_loop<KIND == 2 ? 2 : 4, 16 + 8 * KIND, KIND == 2 ? 0 : 4, 2, 2, 4, 9, TR ? 7 : 6, 5>(c, hook,
    [&](auto sc, auto pc) { constexpr int s = CV(sc), p = CV(pc);
      if constexpr (p == 0) { so = s64 + stg(s) * (TR ? 128 * LD : 256); pins(so); set_m0<(s & 3) * 36864>(m64); }
      if constexpr (p == 4) { so = s80 + stg(s) * (KIND == 2 ? 256 : 20480); pins(so); set_m0<(s & 3) * 36864>(m80); }
      if constexpr (p == 8) set_m0<(s & 3) * 36864 + 4096>(m80); },
    [&](auto, auto pc) { constexpr int p = CV(pc);
      if constexpr (p < 4) dma<0, p * 1024>(v64[p], r64, so); else dma<KIND == 2 ? 0 : 2, (p & 3) * 1024>(v80[p - 4], r80, so); },
    [&](auto kc, auto rc) { constexpr int k = CV(kc), r = CV(rc), kk = k & 3, so_ = ((k >> 2) & 3) * 36864;
      if constexpr (r < 5) { constexpr int o = so_ + 16384 + (KIND == 2 ? (kk >> 1) * 10240 + r * 2048 : (kk * 5 + r) * 1024); dsr16<o % 49152>(f80[k % 3][r], r80a[o / 49152][KIND == 2 ? kk & 1 : 0]); }
      else if constexpr (TR) { constexpr int o = so_ + kk * 4096 + (r - 5) * 512; dsrtr<o % 49152>(ft[k % 3][r - 5], r64a[o / 49152][0]); }
      else { constexpr int o = so_ + (kk >> 1) * 8192; dsr16<o % 49152>(f64[k % 3], r64a[o / 49152][kk & 1]); } },
    [&](auto kc) { constexpr int k = CV(kc);
      sfor<5>([&](auto ic) { pin(f80[k % 3][CV(ic)]); });
      if constexpr (TR) { pin(ft[k % 3][0]); pin(ft[k % 3][1]); f64[k % 3] = cat(ft[k % 3][0], ft[k % 3][1]); } else pin(f64[k % 3]); },
    [&](auto kc, auto mc) { constexpr int k = CV(kc), m = CV(mc);
      const f32x4 z = k == 0 ? f32x4{0, 0, 0, 0} : acc[m];
      acc[m] = KIND == 1 ? mfma(f80[k % 3][m], f64[k % 3], z) : mfma(f64[k % 3], f80[k % 3][m], z); });
  if constexpr (KIND == 2) {
    const int isv = t >= 18, n0 = (t - 18 * isv) * 64 + 16 * w, q = lane >> 4;
    const int vo = ((2 * (w & 1) + (q >> 1)) * 16 + (lane & 15)) * 16 + (q & 1) * 8;
    sfor<5>([&](auto tc) { st8(pack4(acc[CV(tc)]), c.rs, vo, (isv ? O_VC : O_KC) + ((b * 36 + (n0 >> 5)) * 5 + CV(tc)) * 1024); });
  } else if constexpr (KIND == 1) {
    const int T0 = b * 72 + t * 4 + (w & 2);
    const int off = pi_inv_off(w, lane, T0 * 3072, (T0 + 1) * 3072);
    st16(pack8(acc[0], acc[1]), c.rs, off, O_VPT);
    st16(pack8(acc[2], acc[3]), c.rs, off, O_VPT + 1024);
    st16(pack8(acc[4], f32x4{0, 0, 0, 0}), c.rs, off, O_VPT + 2048);
    publish(c, F_X + (2 + b) * 18 + t);
  } else {
    LAS float* red = (LAS float*)(c.lds + LDS_MISC);
    sfor<5>([&](auto tc) { v2u v = pack4(acc[CV(tc)]);
      if constexpr (CV(tc) == 4) { if ((lane & 15) == 13) v = v2u{0x3f803f80u, 0x3f803f80u}; }
      st8(v, c.rs, lane * 16 + (w & 1) * 8, O_WKT + ((b * 36 + 2 * t + (w >> 1)) * 5 + CV(tc)) * 1024);
      const float s = xsum16_32((bflo(v[0]) + bfhi(v[0])) + (bflo(v[1]) + bfhi(v[1])));
      if (lane < 16) red[w * 80 + CV(tc) * 16 + lane] = s; });
    lbar();
    const int tid = w * 64 + lane;
    if (tid < 80) __builtin_amdgcn_raw_buffer_store_b32(asu((red[tid] + red[80 + tid]) + (red[160 + tid] + red[240 + tid])), c.rs, tid * 4, O_U + (b * 18 + t) * 320, SC1);
    publish(c, F_X + b * 18 + t);
  }
}

DEV void gate_strip(Ctx& c, const us* cin, const us* wmod, int strip) {
  const int lane = c.lane, w = c.w, r = lane & 15, q = lane >> 4;
  const rsrc_t rw = mkrs(wmod, D * 6912 * 2), rc = mkrs(cin, 2 * D * 2);
  v4u cv, cv2;
  ald16p<0>(cv, lane * 16, rc, w * 1152);
  ald16p<1024>(cv2, lane < 8 ? lane * 16 : 0x7ff00000, rc, w * 1152);
  const int v64 = tr64_src(lane, w, 13824), s64 = 4608 + strip * 128 + 8 * w * 13824;
  sfor<36>([&](auto pc) { set_m0<CV(pc) * 4096>(c.lb + w * 1024); nop1(); dma<1, 0>(v64, rw, s64 + CV(pc) * 32 * 13824); });
  waitv<36>();
  pin(cv); pin(cv2);
  LAS v4u* sc = (LAS v4u*)(c.lds + LDS_MISC);
  sc[w * 72 + lane] = silu8(cv);
  if (lane < 8) sc[w * 72 + 64 + lane] = silu8(cv2);
  if (lane == 0) sc[288] = v4u{0, 0, 0, 0};
  const unsigned cb = c.lb + LDS_MISC + (r < 2 ? r * 2304 + q * 16 : 4608), cs = r < 2 ? 64 : 0;
  f32x4 acc = {0, 0, 0, 0};
  sfor<3>([&](auto gc) { constexpr int g = CV(gc);
    waitlv<24 - 12 * g>(); bar();
    const unsigned ra = tr64_rd(c.lb + g * 49152, lane, w);
    v2u ft[12][2]; v4u cf[12];
    sfor<12>([&](auto kc) { constexpr int k = CV(kc); dsrtr<k * 4096>(ft[k][0], ra); dsrtr<k * 4096 + 512>(ft[k][1], ra); dsr16<0>(cf[k], cb + (12 * g + k) * cs); });
    waitl();
    sfor<12>([&](auto kc) { constexpr int k = CV(kc); pin(ft[k][0]); pin(ft[k][1]); pin(cf[k]); acc = mfma(cat(ft[k][0], ft[k][1]), cf[k], acc); }); });
  const v2u pv = pack4(acc);
  if (r < 2) st16(v4u{pv[0] << 16, pv[0] & 0xffff0000u, pv[1] << 16, pv[1] & 0xffff0000u}, c.rs, r * 4608 + q * 16, O_GATE + strip * 256 + w * 64);
  publish(c, F_G + strip);
}

template <int N> DEV void ln_load(const Ctx& c, const us* xin, int rt0, int xo) {
  const rsrc_t rx = mkrs(xin, 2048 * D * 2);
  const int v = kc_src<false>(c.lane, D * 2), so = (rt0 * 16 + 8 * (c.w & 1)) * (D * 2) + (c.w >> 1) * 128;
  sfor<9 * N>([&](auto ic) { constexpr int n = CV(ic) / 9, i = CV(ic) % 9; set_m0<n * 36864 + i * 4096>(c.lb + xo + c.w * 1024); nop1(); dma<1, 0>(v, rx, so + n * (16 * D * 2) + i * 256); });
}
template <int N> DEV void ln_read(const Ctx& c, int xo, v4u (&xv)[N][9]) {
  const int w = c.w;
  const unsigned ae = kc_rd(c.lb + xo + ((9 * w) >> 1) * 2048, c.lane, w & 1), ao = kc_rd(c.lb + xo + ((9 * w + 1) >> 1) * 2048, c.lane, (w + 1) & 1);
  sfor<N>([&](auto nc) { constexpr int n = CV(nc); sfor<9>([&](auto kc) { constexpr int k = CV(kc); dsr16<(k >> 1) * 2048>(xv[n][k], ((k & 1) ? ao : ae) + n * 36864); }); });
  waitl();
  sfor<9 * N>([&](auto ic) { pin(xv[CV(ic) / 9][CV(ic) % 9]); });
}
template <int N> DEV void ln_sums(const Ctx& c, v4u (&xv)[N][9]) {
  const int r = c.lane & 15, e = r & 3;
  LAS f32x2* red = (LAS f32x2*)(c.lds + LDS_MISC + 9216);
  f32x4 s1[N], s2[N];
  sfor<9>([&](auto kc) { constexpr int k = CV(kc); sfor<N>([&](auto nc) { constexpr int n = CV(nc); const f32x4 z = {0, 0, 0, 0};
    s1[n] = mfma(v4u{0x3f803f80u, 0x3f803f80u, 0x3f803f80u, 0x3f803f80u}, xv[n][k], k ? s1[n] : z); s2[n] = mfma(xv[n][k], xv[n][k], k ? s2[n] : z); }); });
  sfor<N>([&](auto nc) { constexpr int n = CV(nc);
    const float lo = (e & 1) ? s2[n][1] : s2[n][0], hi = (e & 1) ? s2[n][3] : s2[n][2];
    if ((c.lane >> 4) == (r >> 2)) red[(n * 4 + c.w) * 16 + r] = f32x2{s1[n][0], (e & 2) ? hi : lo}; });
}
template <int N> DEV void ln_norm(const Ctx& c, v4u (&xv)[N][9], f32x2 (&nx)[N][9][4]) {
  const int r = c.lane & 15;
  LAS f32x2* red = (LAS f32x2*)(c.lds + LDS_MISC + 9216);
  sfor<N>([&](auto nc) { constexpr int n = CV(nc);
    const f32x2 s = (red[n * 64 + r] + red[n * 64 + 16 + r]) + (red[n * 64 + 32 + r] + red[n * 64 + 48 + r]);
    const float mean = s[0] * (1.f / D), rs = __builtin_amdgcn_rsqf(fmaxf(s[1] * (1.f / D) - mean * mean, 0.f) + 1e-5f), nm = -mean * rs;
    sfor<9>([&](auto kc) { constexpr int k = CV(kc); for (int e = 0; e < 4; ++e) { nx[n][k][e] = f32x2{bflo(xv[n][k][e]), bfhi(xv[n][k][e])} * rs + nm; pin(nx[n][k][e]); } }); });
}
DEV void mu_poll(const Ctx& c, v2u (&v)[3]) { sfor<3>([&](auto ic) { ald8<O_FL>(v[CV(ic)], (F_MU + CV(ic) * 48 + (c.lane < 48 ? c.lane : 0)) * FS, c.rs, 0); }); }
DEV bool mu_ok(const Ctx& c, v2u (&v)[3]) { pin(v[0]); pin(v[1]); pin(v[2]); return flag_ok(c, v[0]) && flag_ok(c, v[1]) && flag_ok(c, v[2]); }
DEV void mu_spin(const Ctx& c) {
#pragma unroll 1
  for (int i = 0; i < 20000; ++i) { v2u v[3]; mu_poll(c, v); waitv<0>(); if (mu_ok(c, v)) return; __builtin_amdgcn_s_sleep(SLP); }
}
DEV void mod_load(const Ctx& c, int b, v4u (&pv)[3][4]) {
  const int lane = c.lane, mb = O_MODP + b * 36864 + c.w * 1152;
  sfor<4>([&](auto uc) { constexpr int u = CV(uc);
    ald16<0>(pv[0][u], lane * 16, c.rs, mb + u * 9216); ald16<0>(pv[1][u], lane * 16, c.rs, mb + u * 9216 + 4608);
    ald16<1024>(pv[2][u], lane < 16 ? (lane & 7) * 16 + (lane >> 3) * 4608 : 0x7ff00000, c.rs, mb + u * 9216); });
}
template <int N> DEV void ln_apply(const Ctx& c, int rt0, v4u (&pv)[3][4], f32x2 (&nx)[N][9][4]) {
  const int lane = c.lane, w = c.w;
  LAS f32x4* ms = (LAS f32x4*)(c.lds + LDS_MISC + w * 2304);
  sfor<3>([&](auto ic) { constexpr int i = CV(ic);
    sfor<4>([&](auto uc) { pin(pv[i][CV(uc)]); });
    const v2u p = pack4((__builtin_bit_cast(f32x4, pv[i][0]) + __builtin_bit_cast(f32x4, pv[i][1])) + (__builtin_bit_cast(f32x4, pv[i][2]) + __builtin_bit_cast(f32x4, pv[i][3])));
    const f32x4 v = f32x4{bflo(p[0]), bfhi(p[0]), bflo(p[1]), bfhi(p[1])} + (i == 0 ? 0.f : i == 1 ? 1.f : (float)((lane >> 3) & 1));
    if constexpr (i < 2) ms[i * 72 + lane] = v; else if (lane < 16) ms[(lane >> 3) * 72 + 64 + (lane & 7)] = v; });
  waitl();
  const unsigned ma = c.lb + LDS_MISC + w * 2304 + (lane >> 4) * 32;
  v4u mv[3][4];
  auto rdm = [&](auto kc) { sfor<4>([&](auto ic) { dsr16<(CV(ic) >> 1) * 1152 + (CV(ic) & 1) * 16 + CV(kc) * 128>(mv[CV(kc) % 3][CV(ic)], ma); }); };
  rdm(IC(0)); rdm(IC(1));
  const int hb = O_H + 9 * w * 2048;
  sfor<9>([&](auto kc) { constexpr int k = CV(kc);
    waitln<(k < 8 ? 4 : 0)>();
    sfor<4>([&](auto ic) { pin(mv[k % 3][CV(ic)]); });
    if constexpr (k + 2 < 9) rdm(IC(k + 2));
    sfor<N>([&](auto nc) { constexpr int n = CV(nc);
      f32x2 v[4];
      sfor<4>([&](auto ec) { constexpr int e = CV(ec); const v4u t = mv[k % 3][e >> 1], a = mv[k % 3][2 + (e >> 1)];
        v[e] = nx[n][k][e] * f32x2{asf(a[2 * (e & 1)]), asf(a[2 * (e & 1) + 1])} + f32x2{asf(t[2 * (e & 1)]), asf(t[2 * (e & 1) + 1])}; pin(v[e]); });
      st16(v4u{pk2(v[0][0], v[0][1]), pk2(v[1][0], v[1][1]), pk2(v[2][0], v[2][1]), pk2(v[3][0], v[3][1])}, c.rs, lane * 16 + k * 2048, hb + ((rt0 + n) >> 1) * (72 * 1024) + ((rt0 + n) & 1) * 1024); });
    SB(); });
}

enum { OP_OCT, OP_OCT2, OP_A, OP_POLL, OP_ST };
struct Plan {
  static constexpr int MAXO = 260, MAXK = 110;
  int n = 0, kind[MAXO] = {}, idx[MAXO] = {}, wt[MAXO] = {}, need[MAXO] = {};
  int in0[MAXK] = {}, in1[MAXK] = {}, cnt[MAXK] = {}, pre = 0, cnt_pre = 0;
  bool ok = true;
  constexpr void add(int k, int i, int w, int nd) { kind[n] = k; idx[n] = i; wt[n] = w; need[n] = nd; ++n; }
  constexpr void oct(int o, int nd) { add(OP_OCT, o, 1, nd); add(OP_OCT2, o, 1, nd); }
  constexpr int left(int upto, int t) const {
    int last = -1, s = 0;
    for (int i = 0; i < upto; ++i) if (need[i] <= t) last = i;
    for (int i = last + 1; i < upto; ++i) s += wt[i];
    return s;
  }
};
template <int NT> struct P1 {
  static constexpr int NV = 9 - NT, NK = 108, NOCT = 59;
  static constexpr int H0 = 24;
  static constexpr int G_W2 = 16, G_T1 = NT == 4 ? 64 : 60, G_T2 = 80, G_V = 108, FL_T = NT == 4 ? 44 : 48, FL_V = 80;
  static constexpr int nb(int k) { return k < 36 ? NT : k < 72 ? NV : 4; }
  static constexpr int piece(int k, int j) { return k < 36 ? k * NT + j : k < 72 ? 36 * NT + (k - 36) * NV + j : 328 + (k - 72) * 4 + j; }
  static constexpr int kof(int p) { return p < 36 * NT ? p / NT : p < 324 ? 36 + (p - 36 * NT) / NV : p < 328 ? 71 : 72 + (p - 328) / 4; }
  static constexpr bool isbar(int k) { return k % 4 == 0 || k < 4 || k == 70 || k == 74; }
  static constexpr Plan make() {
    Plan p;
    auto need = [](int o) { int k = kof(8 * o) - 2; while (k >= 0 && !isbar(k)) --k; return k < 0 ? -1 : k; };
    auto gate = [](int o) { return o >= 53 ? G_T2 : o >= 41 ? G_T1 : 8 * o + 7 >= 36 * NT ? G_W2 : 0; };
    int o = 0;
    p.add(OP_ST, 2, H0, 0);
    for (; o < 4; ++o) p.oct(o, need(o));
    p.pre = p.n; p.cnt_pre = p.left(p.n, -1);
    int hf = H0;
    for (int k = 0; k < NK; ++k) {
      if (k == 36) p.add(OP_ST, 0, NT, FL_T);
      if (k == 72) p.add(OP_ST, 1, NV, FL_V);
      p.cnt[k] = p.left(p.n, k);
      p.in0[k] = p.n;
      if (k == G_W2 - 4) p.add(OP_POLL, 0, 1, G_W2);
      if (k == G_T1 - 5) p.add(OP_POLL, 1, 1, G_T1);
      if (k == G_T2 - 5) p.add(OP_POLL, 2, 1, G_T2);
      if (k == G_V - 6) p.add(OP_POLL, 3, 1, G_V);
      for (int i = 0; i < 2 && hf < 72; ++i, ++hf) p.add(OP_A, hf, 1, hf / 2);
      if (o < NOCT && gate(o) <= k && (hf == 72 || need(o) <= k + 5)) {
        bool free_ = o < 18;
        if (!free_) for (int kb = kof(8 * (o - 18) + 7); kb <= k; ++kb) if (isbar(kb)) free_ = true;
        if (free_) { if (need(o) <= k) p.ok = false; p.oct(o, need(o)); ++o; }
      }
      p.in1[k] = p.n;
      if (p.cnt[k] > 60) p.ok = false;
    }
    if (o != NOCT) p.ok = false;
    return p;
  }
};
struct P3 {
  static constexpr int NK = 32, DIST = 5, AW = 7;
  static constexpr bool isbar(int k) { return k % 2 == 1; }
  static constexpr Plan make() {
    Plan p;
    for (int k = 0; k < DIST; ++k) p.add(OP_A, k, 1, k);
    p.pre = p.n;
    int o = 18;
    for (int k = 0; k < NK; ++k) {
      p.cnt[k] = p.left(p.n, k);
      p.in0[k] = p.n;
      if (k + DIST < NK) p.add(OP_A, k + DIST, 1, k + DIST);
      if (o < 40) {
        bool free_ = false;
        for (int kb = (8 * (o - 18) + 7) / 9; kb <= k; ++kb) if (isbar(kb)) free_ = true;
        int nd = o < 36 ? (8 * o) / 9 - 2 : NK;
        while (nd < NK && !isbar(nd)) --nd;
        if (free_) { if (nd <= k) p.ok = false; p.oct(o, nd); ++o; }
      }
      p.in1[k] = p.n;
    }
    if (o != 40) p.ok = false;
    return p;
  }
};

template <int PAR> DEV void k3_epi(const Ctx& c, f32x4 (&o)[9], f32x4 (&xf)[10], v4u (&gv)[10], int nb, int qt) {
  const int lane = c.lane, vo = lane * 16, ks0 = (9 * nb) >> 1;
  istub<79 - PAR>();
  v4u wk[5][5];
  sfor<25>([&](auto ic) { dsr16<CV(ic) * 1024>(wk[CV(ic) / 5][CV(ic) % 5], c.lb + vo); });
  v4u x2[5];
  sfor<5>([&](auto uc) { constexpr int u = CV(uc);
    constexpr int tl = PAR ? 2 * u - 1 : 2 * u, th = tl + 1;
    v2u lo = {0, 0}, hi = {0, 0};
    if constexpr (tl >= 0) lo = pack4(xf[2 * u] + __builtin_bit_cast(f32x4, gv[2 * u]) * o[tl]);
    if constexpr (th <= 8) hi = pack4(xf[2 * u + 1] + __builtin_bit_cast(f32x4, gv[2 * u + 1]) * o[th]);
    x2[u] = cat(lo, hi);
    const int so = O_X2 + (qt * 36 + ks0 + u) * 1024;
    if constexpr (tl >= 0 && th <= 8) st16(x2[u], c.rs, vo, so);
    else if constexpr (tl >= 0) st8(lo, c.rs, vo, so);
    else st8(hi, c.rs, vo + 8, so); });
  waitl();
  sfor<25>([&](auto ic) { pin(wk[CV(ic) / 5][CV(ic) % 5]); });
  f32x4 s[6];
  sfor<5>([&](auto uc) { constexpr int u = CV(uc);
    sfor<5>([&](auto tc) { constexpr int t = CV(tc); s[t] = mfma(wk[u][t], x2[u], u == 0 ? f32x4{0, 0, 0, 0} : s[t]); });
    s[5] = mfma(x2[u], x2[u], u == 0 ? f32x4{0, 0, 0, 0} : s[5]); SB(); });
  sfor<6>([&](auto tc) { pin(s[CV(tc)]); });
  mfma_settle();
  sfor<5>([&](auto tc) { st16(__builtin_bit_cast(v4u, s[CV(tc)]), c.rs, vo, O_SCP + ((qt * 8 + nb) * 5 + CV(tc)) * 1024); });
  const int r = lane & 15, e = r & 3;
  if ((lane >> 4) == (r >> 2)) __builtin_amdgcn_raw_buffer_store_b32(asu(e == 0 ? s[5][0] : e == 1 ? s[5][1] : e == 2 ? s[5][2] : s[5][3]), c.rs, r * 4, O_LNP + (qt * 8 + nb) * 64, SC1);
}

template <int PAR> DEV void k4_out(v4u (&vp)[9][3], v4u (&pc)[3], v4u (&x2)[5], rsrc_t ro, int nb, int qt, int lane) {
  const int r = lane & 15, q = lane >> 4, ks0 = (9 * nb) >> 1;
  f32x4 o[9];
  sfor<9>([&](auto tc) { constexpr int t = CV(tc); o[t] = mfma(vp[t][2], pc[2], mfma(vp[t][1], pc[1], mfma(vp[t][0], pc[0], f32x4{0, 0, 0, 0}))); });
  const int oo = (qt * 16 + r) * (D * 2) + ks0 * 64 + q * 16;
  sfor<5>([&](auto uc) { constexpr int u = CV(uc); constexpr int tl = PAR ? 2 * u - 1 : 2 * u, th = tl + 1;
    v4u y = {0, 0, 0, 0};
    if constexpr (tl >= 0) { y[0] = pk2(bflo(x2[u][0]) + o[tl][0], bfhi(x2[u][0]) + o[tl][1]); y[1] = pk2(bflo(x2[u][1]) + o[tl][2], bfhi(x2[u][1]) + o[tl][3]); }
    if constexpr (th <= 8) { y[2] = pk2(bflo(x2[u][2]) + o[th][0], bfhi(x2[u][2]) + o[th][1]); y[3] = pk2(bflo(x2[u][3]) + o[th][2], bfhi(x2[u][3]) + o[th][3]); }
    if constexpr (tl >= 0 && th <= 8) st16o<u * 64>(y, ro, oo);
    else if constexpr (tl >= 0) __builtin_amdgcn_raw_buffer_store_b64(v2u{y[0], y[1]}, ro, oo, u * 64, OAUX);
    else __builtin_amdgcn_raw_buffer_store_b64(v2u{y[2], y[3]}, ro, oo + 8, u * 64, OAUX); });
}

template <int NT> DEV void tv_scores(const Ctx& c, int mi, int ci, f32x4 (&sacc)[2][4], v2u& pollv) {
  using P = P1<NT>;
  constexpr int NV = 9 - NT;
  constexpr Plan pl = P::make();
  static_assert(pl.ok, "schedule");
  const int lane = c.lane, w = c.w, vo = lane * 16, b = mi >> 3, qb = b * 16 + ci;
  v4u h[72];
  const int t0 = NT == 4 ? ci * 4 : 48 + (ci - 12) * 6, v0 = NT == 4 ? ci * 5 : 60 + (ci - 12) * 3;
  const int l9 = lane < 18 ? lane : 0;
  const int fw1 = F_W + ((l9 < 9 ? t0 : t0 + NT - 1) >> 3) * 9 + l9 % 9;
  int pf[4];
  pf[0] = F_W + 81 + ((l9 < 9 ? v0 : v0 + NV - 1) >> 3) * 9 + l9 % 9;
  pf[1] = F_T + (qb >> 1) * 16 + (lane < 12 ? lane : 0);
  pf[2] = F_T + (qb >> 1) * 16 + 12 + (lane & 3);
  { const int nb = mi & 7, s0 = nb < 7 ? (9 * nb) / 5 : 13;
    pf[3] = lane < 24 ? F_V + (b * 16 + s0 + (lane >> 3)) * 8 + (lane & 7) : lane < 42 ? F_X + b * 18 + lane - 24 : lane < 60 ? F_G + lane - 42 : F_G; }
  const int sw = O_WT + ci * (324 * 1024) + w * 2048, st = O_T + qb * (144 * 1024) + w * 2048 - 41 * 8192;
  const unsigned mw = c.lb + w * 2048;
  unsigned rd[3];
  rd[0] = c.lb + vo; rd[1] = rd[0] + 49152; rd[2] = rd[0] + 98304;
  v4u bf[3][6];
  const int rot = (mi + ci) % 3;
  int dr[3];
  sfor<3>([&](auto tc) { dr[CV(tc)] = (CV(tc) + rot) % 3 - CV(tc); });
  const int hb = O_H + (mi * 4 + w) * (72 * 1024);
  int so, sh;
  auto hprep = [&](auto fc) { constexpr int f = CV(fc); if constexpr ((f & 3) == 0) { sh = hb + (f >> 2) * 4096; if constexpr (f < 48) sh += dr[f / 16] * 16384; pins(sh); } };
  auto hload = [&](auto fc) { constexpr int f = CV(fc); if constexpr (f < 64) ald16a<(f & 3) * 1024>(h[f], vo, c.rs, sh); else ald16<(f & 3) * 1024>(h[f], vo, c.rs, sh); };
  auto prep = [&](auto oc) { constexpr int kind = pl.kind[CV(oc)], idx = pl.idx[CV(oc)];
    if constexpr (kind == OP_OCT) {
      constexpr int o0 = idx >= 41 ? 41 : 8 * idx >= 36 * NT ? 36 * NT / 8 : 0, n8 = idx >= 41 ? 4 : o0 ? NV : NT;
      so = (idx < 41 ? sw : st) + idx * 8192;
      if constexpr (idx - o0 < 3 * n8) so += dr[(idx - o0) / n8] * (n8 * 8192);
      pins(so); set_m0<((8 * idx) % 144) * 1024>(mw); }
    if constexpr (kind == OP_A) hprep(IC(idx)); };
  auto op = [&](auto oc) { constexpr int kind = pl.kind[CV(oc)], idx = pl.idx[CV(oc)];
    if constexpr (kind == OP_POLL) ald8<O_FL>(pollv, pf[idx] * FS, c.rs, 0);
    else if constexpr (kind == OP_A) hload(IC(idx));
    else if constexpr (kind != OP_ST) dma<2, (kind == OP_OCT2) * 1024>(vo, c.rs, so); };
  auto rdb = [&](auto kc, auto jc) { constexpr int p = P::piece(CV(kc), CV(jc)) % 144; dsr16<(p % 48) * 1024>(bf[CV(kc) % 3][CV(jc)], rd[p / 48]); };
  {
    bool wok = false;
#pragma unroll 1
    for (int i = 0; i < 20000; ++i) {
      v2u v;
      ald8<O_FL>(v, (lane < 18 ? fw1 : F_H + mi * 8 + 2 * w + (lane & 1)) * FS, c.rs, 0);
      waitv<0>(); pin(v);
      const unsigned long long bad = __ballot(v[0] != c.t0 || v[1] != c.t1);
      wok = !(bad & 0x3ffff);
      if (!(bad >> 18)) break;
      __builtin_amdgcn_s_sleep(SLP);
    }
    sfor<P::H0>([&](auto fc) { hprep(fc); hload(fc); });
    iprobe<(NT == 4 ? 40 : 56), (NT == 4 ? 51 : 67)>(w);
    iprobe<72, 73>(w);
    STAMP(1, 1);
    if (!wok) flag_spin(c, fw1);
  }
  STAMP(1, 2);
  sfor<pl.pre>([&](auto oc) { prep(oc); nop1(); op(oc); });
  waitv<pl.cnt_pre>(); bar();
  sfor<2 * NT>([&](auto jc) { rdb(IC(CV(jc) / NT), IC(CV(jc) % NT)); });
  SB();
  f32x4 acc[2][6];
  sfor<108>([&](auto kc) {
    constexpr int k = CV(kc), ks = k % 36, NB = P::nb(k), NM = 2 * NB;
    if constexpr (k == 36) {
      STAMP(1, 6);
      mfma_settle();
      const int tb = O_T + (mi * 2 + (w >> 1)) * (144 * 1024) + (w & 1) * 2048 + (t0 >> 1) * 4096;
      sfor<2>([&](auto ic) { sfor<NT / 2>([&](auto uc) { constexpr int i = CV(ic), u = CV(uc); st16(pack8(acc[i][2 * u], acc[i][2 * u + 1]), c.rs, vo, tb + u * 4096 + i * 1024); }); });
      asm volatile("s_nop 1"); SB();
    }
    if constexpr (k == 72) {
      STAMP(1, 10);
      mfma_settle();
      sfor<NV>([&](auto jc) { constexpr int j = CV(jc); const int T = v0 + j, nb = T / 9;
        st16(pack8(acc[0][j], acc[1][j]), c.rs, vo, O_VT + (((b * 8 + nb) * 32 + (mi & 7) * 4 + w) * 9 + T - 9 * nb) * 1024); });
      asm volatile("s_nop 1"); SB();
    }
    if constexpr (k == 0 || k == 12 || k == 24) STAMP(1, 3 + k / 12);
    if constexpr (k == 36 || k == 48 || k == 60) STAMP(1, 7 + (k - 36) / 12);
    if constexpr (k == 72 || k == 84 || k == 96) STAMP(1, 11 + (k - 72) / 12);
    if constexpr (k % 10 == 0) istub<(NT == 4 ? 40 : 56) + k / 10>();
    waitvl<pl.cnt[k], (k + 1 < 108 ? P::nb(k + 1) : 0)>();
    if constexpr (P::isbar(k)) bar();
    if constexpr (k == P::G_W2 || k == P::G_T1 || k == P::G_T2) {
      pin(pollv);
      if (!flag_ok(c, pollv)) flag_spin(c, pf[k == P::G_W2 ? 0 : k == P::G_T1 ? 1 : 2]);
    }
    if constexpr (k == P::FL_T) { if (w == 0) flag_set(c, F_T + mi * 16 + ci); }
    if constexpr (k == P::FL_V) { if (w == 0) flag_set(c, F_V + (b * 16 + ci) * 8 + (mi & 7)); }
    sfor<NB>([&](auto jc) { pin(bf[k % 3][CV(jc)]); });
    SB();
    kstep<NM, (k + 2 < 108 ? P::nb(k + 2) : 0), pl.in1[k] - pl.in0[k]>(
      [&](auto mc) { constexpr int m = CV(mc), j = m >> 1, i = m & 1, f = 2 * ks + i;
        if constexpr (k < 72) mma_h<(f < 64), (k >= 36), ks == 0>(acc[i][j], h[f], bf[k % 3][j]);
        else mma_h<(f < 64), true, ks == 0>(sacc[i][j], h[f], bf[k % 3][j]); },
      [&](auto rc) { rdb(IC(k + 2), rc); }, [&](auto qc) { prep(IC(pl.in0[k] + CV(qc))); }, [&](auto qc) { op(IC(pl.in0[k] + CV(qc))); });
  });
  STAMP(1, 14);
  mfma_settle();
  waitv<0>();
  pin(pollv);
  if (!flag_ok(c, pollv)) flag_spin(c, pf[3]);
}

__global__ __launch_bounds__(256, 1) void fused(char* ws, const us* xin, const us* cin, const us* y, const us* wqkv, const us* wo, const us* wq_c,
                                                const us* wkv, const us* wo_c, const us* wmod, us* out) {
  extern __shared__ __attribute__((aligned(16))) char smem[];
  Ctx c;
  c.ws = ws; c.rs = mkrs(ws, WS_BYTES); c.lds = (LAS char*)smem; c.lb = (unsigned)(size_t)c.lds;
  c.lane = threadIdx.x & 63; c.w = __builtin_amdgcn_readfirstlane(threadIdx.x >> 6);
  const int lane = c.lane, w = c.w, vo = lane * 16;
  STAMP(0, 0);
  asm volatile("s_mov_b32 m0, %0\n\ts_nop 0\n\tbuffer_load_dword %1, %2, 0 offen sc1 lds" ::"s"(c.lb + LDS_BYTES - 512 - w * 256), "v"(O_EP + lane * 4), "s"(c.rs) : "memory");
  const int x = blockIdx.x & 7, l = blockIdx.x >> 3;
  if constexpr (PREC) {
    unsigned long long pc;
    unsigned len;
    asm volatile("s_getpc_b64 %0\n.Lpc0:\n\ts_mov_b32 %1, .Lend0-.Lpc0" : "=s"(pc), "=s"(len));
    if (w == 3) touch(c.lb, (l * 64 + lane) * 128, mkrs((const void*)pc, len), 0);
  }
  {
    const int q = x & 3, hx = x >> 2, ni = (x & 1) ? 4 : 5, nj = (x & 2) ? 4 : 5, nw = ni * nj;
    if (l < nw) {
      const int ti = (x & 1) * 5 + l % ni, tj = ((x >> 1) & 1) * 5 + l / ni, mu = ((q == 0 ? 0 : q == 1 ? 25 : q == 2 ? 45 : 65) + l) * 2 + hx;
      if (x < 4) w_tile<0>(c, wqkv, wo, cin, wmod, ti, tj, l % ni, ni, l / ni, nj, mu); else w_tile<1>(c, wqkv, wo, cin, wmod, ti, tj, l % ni, ni, l / ni, nj, mu);
    } else {
      const int e = l - nw, nk = q == 0 ? 6 : q == 1 ? 10 : q == 2 ? 8 : 12;
      if (e < nk) {
        const int t = hx * 18 + (q == 0 ? 0 : q == 1 ? 3 : q == 2 ? 8 : 12) + (e >> 1), bb = e & 1, rot = ((e >> 1) + 5 * bb) % 9;
        const int rt = bb * 64 + 33 + t;
        const bool ln = t < 31;
        v4u xv[1][9], pv[3][4];
        f32x2 nx[1][9][4];
        v2u mp[3];
        ln_load<1>(c, xin, ln ? rt : 128, 110592);
        small_tile<2>(c, wkv, y, bb, t, rot, [&](auto kc) { constexpr int k = CV(kc);
          if constexpr (k == -1) { iprobe<33, 38>(w); if (hx) iprobe<24, 30>(w); else iprobe<16, 22>(w); }
          if constexpr (k == -2) { c.make_tag(); ln_read<1>(c, 110592, xv); ln_sums<1>(c, xv); waitl(); }
          if constexpr (k == 3) ln_norm<1>(c, xv, nx);
          if constexpr (k == 27) mu_poll(c, mp);
          if constexpr (k == 31) { if (!mu_ok(c, mp)) mu_spin(c); mod_load(c, bb, pv); } });
        STAMP(3, 1);
        waitv<5>();
        if (ln) ln_apply<1>(c, rt, pv, nx); else waitv<0>();
        STAMP(3, 2);
        waitv<9>(); bar();
        if (w == 0) flag_set(c, F_KVC + (hx * 2 + bb) * 18 + t - 18 * hx);
        STAMP(4, 0);
        auto gate = [&](auto kc) { if constexpr (CV(kc) == -1) {
          waitv<8>(); bar();
          if (w == 0 && ln) flag_set(c, F_H + rt);
          flag_spin(c, F_KVC + (hx * 2 + bb) * 18 + (lane < 18 ? lane : 0)); } };
        if (hx) small_tile<1>(c, wo_c, y, bb, t - 18, rot, gate); else small_tile<0>(c, wq_c, y, bb, t, rot, gate);
      } else {
        const int a2 = (q == 0 ? 0 : q == 1 ? 1 : q == 2 ? 3 : 7) + e - nk, rt = hx * 64 + 3 * a2;
        v4u xv[3][9], pv[3][4];
        f32x2 nx[3][9][4];
        ln_load<3>(c, xin, rt, 0);
        waitv<0>(); c.make_tag(); bar();
        ln_read<3>(c, 0, xv); ln_sums<3>(c, xv);
        lbar();
        ln_norm<3>(c, xv, nx);
        STAMP(3, 3);
        mu_spin(c);
        STAMP(3, 4);
        mod_load(c, hx, pv);
        waitv<0>();
        ln_apply<3>(c, rt, pv, nx);
        STAMP(3, 5);
        waitv<0>(); bar();
        if (w == 0 && lane < 3) __builtin_amdgcn_raw_buffer_store_b64(c.tag(), c.rs, lane * FS, O_FL + (F_H + rt) * FS, SC1);
        if (a2 < 9) gate_strip(c, cin, wmod, a2 * 2 + hx);
      }
    }
  }
  STAMP(1, 0);
  c.t0 = __builtin_amdgcn_readfirstlane(c.t0); c.t1 = __builtin_amdgcn_readfirstlane(c.t1);
  const int mi = MAP48 ? (x & 3) * 4 + (l & 3) : (x & 1) * 8 + (l & 7), ci = MAP48 ? (x >> 2) * 8 + (l >> 2) : (x >> 1) * 4 + (l >> 3), b = mi >> 3, kb = mi & 7, qb = b * 16 + ci;
  f32x4 sacc[2][4];
  v2u pollv;
  if (ci < 12) tv_scores<4>(c, mi, ci, sacc, pollv); else tv_scores<6>(c, mi, ci, sacc, pollv);
  istub<72>();
  STAMP(5, 0);
  const int nb = kb, qt = qb * 4 + w;
  const int sv = O_VT + (b * 8 + nb) * (288 * 1024) + w * 2048, rot = (ci + nb) & 3;
  auto voct = [&](int o) { const int u = o + 9 * rot; return sv + (u >= 36 ? u - 36 : u) * 8192; };
  const unsigned mw = c.lb + w * 2048;
  {
    LAS float* red = (LAS float*)(c.lds + LDS_MISC);
    const float cs = 0.029462782549439476f * 1.4426950408889634f;
    float mx[4], sm[4];
    sfor<4>([&](auto jc) { constexpr int j = CV(jc);
      mx[j] = max3(max3(sacc[0][j][0], sacc[0][j][1], sacc[0][j][2]), max3(sacc[0][j][3], sacc[1][j][0], sacc[1][j][1]), max3(sacc[1][j][2], sacc[1][j][3], sacc[1][j][3])); });
    red[lane * 4 + w] = xred4(mx[0], mx[1], mx[2], mx[3], FMax{});
    lbar();
    const f32x4 own = *(const LAS f32x4*)(red + lane * 4);
    sfor<4>([&](auto jc) { constexpr int j = CV(jc);
      const f32x4 t = *(const LAS f32x4*)(red + (j * 16 + (lane & 15)) * 4);
      const float m = max4(t) * cs;
      f32x4 p0, p1;
      for (int r = 0; r < 4; ++r) { p0[r] = __builtin_amdgcn_exp2f(sacc[0][j][r] * cs - m); p1[r] = __builtin_amdgcn_exp2f(sacc[1][j][r] * cs - m); }
      st16(pack8(p0, p1), c.rs, vo, O_P + ((qb * 4 + j) * 32 + kb * 4 + w) * 1024);
      sm[j] = ((p0[0] + p0[1]) + (p0[2] + p0[3])) + ((p1[0] + p1[1]) + (p1[2] + p1[3])); });
    st8(v2u{asu(max4(own) * cs), asu(xred4(sm[0], sm[1], sm[2], sm[3], FAdd{}))}, c.rs,
        (lane >> 4) * 4096 + (lane & 15) * 8, O_ST + qb * 16384 + kb * 512 + w * 128);
    SB();
  }
  auto vburst = [&](auto oc) { const int so = voct(CV(oc)); set_m0<CV(oc) * 8192>(mw); nop1(); dma<2, 0>(vo, c.rs, so); dma<2, 1024>(vo, c.rs, so); };
  sfor<4>(vburst);
  waitv<8>();
  flag_set(c, F_P + qb * 32 + kb * 4 + w);
  STAMP(5, 1);
  v4u xv[5], gv[10];
  { const rsrc_t rx = mkrs(xin, 2048 * D * 2);
    const int ks0 = (9 * nb) >> 1;
    sfor<5>([&](auto uc) { ald16p<CV(uc) * 64>(xv[CV(uc)], (lane & 15) * (D * 2) + (lane >> 4) * 16, rx, qt * (16 * D * 2) + ks0 * 64); });
    sfor<10>([&](auto uc) { ald16<(CV(uc) >> 1) * 128 + (CV(uc) & 1) * 16>(gv[CV(uc)], (lane >> 4) * 32, c.rs, O_GATE + b * 4608 + ks0 * 128); }); }
  srange<4, 18>(vburst);
  iprobe<73, 78>(w);
  if (nb & 1) iprobe<78, 79>(w); else iprobe<79, 80>((w + 1) & 3);
  iprobe<80, 84>(w);
  flag_spin(c, F_P + qb * 32 + (lane & 31));
  STAMP(5, 2);
  istub<73>();
  f32x4 xf[10];
  sfor<5>([&](auto uc) { constexpr int u = CV(uc); pin(xv[u]);
    xf[2 * u] = f32x4{bflo(xv[u][0]), bfhi(xv[u][0]), bflo(xv[u][1]), bfhi(xv[u][1])}; xf[2 * u + 1] = f32x4{bflo(xv[u][2]), bfhi(xv[u][2]), bflo(xv[u][3]), bfhi(xv[u][3])}; });
  sfor<10>([&](auto uc) { pin(gv[CV(uc)]); });
  f32x4 tot[9];
  {
    constexpr Plan pl = P3::make();
    static_assert(pl.ok, "schedule");
    v2u sts[8];
    sfor<8>([&](auto ic) { ald8<CV(ic) * 128>(sts[CV(ic)], (((lane >> 4) + rot) & 3) * 1024 + (lane & 15) * 8, c.rs, O_ST + qt * 4096); });
    const int sk = O_WKT + (b * 36 + ((9 * nb) >> 1)) * 5120 + w * 2048 - 36 * 8192, ab = O_P + qt * 32768;
    unsigned rd[3];
    rd[0] = c.lb + vo; rd[1] = rd[0] + 49152; rd[2] = rd[0] + 98304;
    v4u a[P3::AW], bf[3][9];
    f32x4 acc[9];
    float f[8];
    int so, sa;
    auto prep = [&](auto oc) { constexpr int kind = pl.kind[CV(oc)], idx = pl.idx[CV(oc)];
      if constexpr (kind == OP_OCT) { so = idx < 36 ? voct(idx) : sk + idx * 8192; pins(so); set_m0<((8 * idx) % 144) * 1024>(mw); }
      if constexpr (kind == OP_A && (idx & 3) == 0) { sa = ab + (((idx >> 2) + 2 * rot) & 7) * 4096; pins(sa); } };
    auto op = [&](auto oc) { constexpr int kind = pl.kind[CV(oc)], idx = pl.idx[CV(oc)];
      if constexpr (kind == OP_A) ald16<(idx & 3) * 1024>(a[idx % P3::AW], vo, c.rs, sa); else dma<2, (kind == OP_OCT2) * 1024>(vo, c.rs, so); };
    auto rdb = [&](auto kc, auto jc) { constexpr int p = (CV(kc) * 9 + CV(jc)) % 144; dsr16<(p % 48) * 1024>(bf[CV(kc) % 3][CV(jc)], rd[p / 48]); };
    sfor<pl.pre>([&](auto oc) { prep(oc); op(oc); });
    bar();
    sfor<18>([&](auto jc) { rdb(IC(CV(jc) / 9), IC(CV(jc) % 9)); });
    SB();
    sfor<32>([&](auto kc) {
      constexpr int k = CV(kc);
      if constexpr (k % 8 == 0 || k == 1) STAMP(5, k == 1 ? 4 : k == 0 ? 3 : 4 + k / 8);
      if constexpr (k % 8 == 4) istub<74 + k / 8>();
      waitvl<pl.cnt[k], (k + 1 < 32 ? 9 : 0)>();
      if constexpr (P3::isbar(k)) bar();
      sfor<9>([&](auto jc) { pin(bf[k % 3][CV(jc)]); });
      pin(a[k % P3::AW]);
      if constexpr (k == 1) {
        sfor<8>([&](auto ic) { pin(sts[CV(ic)]); });
        const float m0 = asf(sts[0][0]), m1 = asf(sts[4][0]), m = xmax16_32(max3(m0, m1, m1));
        const float e0 = __builtin_amdgcn_exp2f(m0 - m), e1 = __builtin_amdgcn_exp2f(m1 - m);
        const float s = __builtin_amdgcn_rcpf(xsum16_32(e0 * ((asf(sts[0][1]) + asf(sts[1][1])) + (asf(sts[2][1]) + asf(sts[3][1]))) + e1 * ((asf(sts[4][1]) + asf(sts[5][1])) + (asf(sts[6][1]) + asf(sts[7][1])))));
        rows4(e0 * s, f[0], f[2], f[4], f[6]); rows4(e1 * s, f[1], f[3], f[5], f[7]);
      }
      SB();
      kstep<9, (k + 2 < 32 ? 9 : 0), pl.in1[k] - pl.in0[k]>(
        [&](auto mc) { constexpr int m = CV(mc);
          if constexpr (k % 4 == 0 && k > 0) { if constexpr (k == 4) tot[m] = acc[m] * f[0]; else tot[m] += acc[m] * f[k / 4 - 1]; pin(tot[m]); }
          mma_h<false, false, k % 4 == 0>(acc[m], a[k % P3::AW], bf[k % 3][m]); },
        [&](auto rc) { rdb(IC(k + 2), rc); }, [&](auto qc) { prep(IC(pl.in0[k] + CV(qc))); }, [&](auto qc) { op(IC(pl.in0[k] + CV(qc))); });
    });
    mfma_settle();
    sfor<9>([&](auto jc) { tot[CV(jc)] += acc[CV(jc)] * f[7]; });
  }
  STAMP(5, 8);
  waitv<0>(); bar();
  if (nb & 1) k3_epi<1>(c, tot, xf, gv, nb, qt); else k3_epi<0>(c, tot, xf, gv, nb, qt);
  const int qt4 = qb * 4 + (nb >> 1), nb4 = (nb & 1) * 4 + w;
  SB();
  v4u vp[9][3];
  flag_spin(c, F_X + 36 + b * 18 + (lane < 18 ? lane : 0));
  flag_set(c, F_S + qt * 8 + nb);
  STAMP(5, 9);
  istub<80>();
  sfor<27>([&](auto ic) { constexpr int i = CV(ic); ald16<(i & 3) * 1024>(vp[i / 3][i % 3], vo, c.rs, O_VPT + (b * 72 + 9 * nb4) * 3072 + (i >> 2) * 4096); });
  LAS float* ured = (LAS float*)(c.lds + LDS_MISC + 2048);
  { const int tid = w * 64 + lane;
    if (tid < 240) { const int g = tid / 80, m = tid % 80; float u = 0.f;
      for (int i = 0; i < 6; ++i) u += asf(__builtin_amdgcn_raw_buffer_load_b32(c.rs, (g * 6 + i) * 320 + m * 4, O_U + b * 5760, SC1));
      ured[tid] = u; } }
  if (blockIdx.x == 0 && w == 3) {
    sfor<4>([&](auto ic) { flag_spin(c, F_T + CV(ic) * 64 + lane); });
    if (lane == 0) __builtin_amdgcn_raw_buffer_store_b64(c.tag(), c.rs, 0, O_EP, SC1);
  }
  flag_spin(c, F_S + qt4 * 8 + (lane & 7));
  STAMP(5, 10);
  istub<81>();
  {
    const int r = lane & 15, q = lane >> 4;
    v4u sp[2][5], x2[5];
    float lp0, lp1;
    sfor<10>([&](auto ic) { constexpr int i = CV(ic); ald16<(i & 3) * 1024>(sp[i / 5][i % 5], vo, c.rs, O_SCP + (qt4 * 8 + 2 * w) * 5120 + (i >> 2) * 4096); });
    asm volatile("buffer_load_dword %0, %2, %3, %4 offen sc1\n\tbuffer_load_dword %1, %2, %3, %4 offen offset:64 sc1" : "=&v"(lp0), "=&v"(lp1) : "v"(q * 128 + r * 4), "s"(c.rs), "s"(O_LNP + qt4 * 512));
    sfor<5>([&](auto uc) { constexpr int u = CV(uc); ald16<(u & 3) * 1024>(x2[u], vo, c.rs, O_X2 + (qt4 * 36 + ((9 * nb4) >> 1)) * 1024 + (u >> 2) * 4096); });
    waitv<5>();
    sfor<10>([&](auto ic) { pin(sp[CV(ic) / 5][CV(ic) % 5]); });
    pin(lp0); pin(lp1);
    LAS f32x4* red = (LAS f32x4*)(c.lds + 32768);
    sfor<5>([&](auto tc) { constexpr int t = CV(tc); red[(w * 5 + t) * 64 + lane] = __builtin_bit_cast(f32x4, sp[0][t]) + __builtin_bit_cast(f32x4, sp[1][t]); });
    const float sq = xsum16_32(lp0 + lp1) * (1.f / D);
    lbar();
    f32x4 s[5];
    float mx = -3.0e38f;
    sfor<5>([&](auto tc) { constexpr int t = CV(tc); s[t] = (red[t * 64 + lane] + red[(5 + t) * 64 + lane]) + (red[(10 + t) * 64 + lane] + red[(15 + t) * 64 + lane]); });
    const float mean = xsum16_32(q == 3 ? s[4][1] : 0.f) * (1.f / D);
    const float cc = 0.029462782549439476f * 1.4426950408889634f * __builtin_amdgcn_rsqf(fmaxf(sq - mean * mean, 0.f) + 1e-5f);
    sfor<5>([&](auto tc) { constexpr int t = CV(tc);
      const LAS f32x4* u4 = (const LAS f32x4*)(ured + t * 16 + q * 4);
      s[t] = (s[t] - ((u4[0] + u4[20]) + u4[40]) * mean) * cc;
      if constexpr (t == 4) for (int e = 1; e < 4; ++e) s[t][e] = q == 3 ? -3.0e38f : s[t][e];
      mx = max3(max3(mx, s[t][0], s[t][1]), s[t][2], s[t][3]); });
    istub<82>();
    mx = xmax16_32(mx);
    float sum = 0.f;
    sfor<5>([&](auto tc) { constexpr int t = CV(tc); for (int e = 0; e < 4; ++e) { s[t][e] = __builtin_amdgcn_exp2f(s[t][e] - mx); sum += s[t][e]; } });
    sum = __builtin_amdgcn_rcpf(xsum16_32(sum));
    v4u pc[3];
    pc[0] = pack8(s[0] * sum, s[1] * sum); pc[1] = pack8(s[2] * sum, s[3] * sum); pc[2] = pack8(s[4] * sum, f32x4{0, 0, 0, 0});
    waitv<0>();
    sfor<27>([&](auto ic) { pin(vp[CV(ic) / 3][CV(ic) % 3]); });
    sfor<5>([&](auto uc) { pin(x2[CV(uc)]); });
    istub<83>();
    asm volatile(".Lend0:");
    const rsrc_t ro = mkrs(out, 2048 * D * 2);
    if (w & 1) k4_out<1>(vp, pc, x2, ro, nb4, qt4, lane); else k4_out<0>(vp, pc, x2, ro, nb4, qt4, lane);
  }
}
}

extern "C" void kernel_launch(void* const* d_in, const int* in_sizes, int n_in, void* d_out, int out_size, void* d_ws, size_t ws_size, hipStream_t stream) {
  (void)hipFuncSetAttribute((const void*)fused, hipFuncAttributeMaxDynamicSharedMemorySize, LDS_BYTES);
  fused<<<256, 256, LDS_BYTES, stream>>>((char*)d_ws + (WSEND ? ((ws_size - WS_BYTES) & ~(size_t)0xfffff) : 0), (const us*)d_in[0], (const us*)d_in[1], (const us*)d_in[2], (const us*)d_in[3], (const us*)d_in[4],
                                         (const us*)d_in[5], (const us*)d_in[6], (const us*)d_in[7], (const us*)d_in[8], (us*)d_out);
}
```

```cpp
#include <hip/hip_runtime.h>
#include <utility>
#include <stdint.h>

#define NTPOL 1
#define WSEND 0
#define STAG 3
#define PREC 1
#define PRET 0
#define WNT 0
#define FS 64
#define SLP 2
#define MAP48 1
namespace {
typedef unsigned v4u __attribute__((ext_vector_type(4)));
typedef unsigned v2u __attribute__((ext_vector_type(2)));
typedef float f32x4 __attribute__((ext_vector_type(4)));
typedef float f32x2 __attribute__((ext_vector_type(2)));
typedef short bf16x8 __attribute__((ext_vector_type(8)));
typedef __bf16 bf16x2_t __attribute__((ext_vector_type(2)));
typedef __amdgpu_buffer_rsrc_t rsrc_t;
typedef unsigned short us;
#define LAS __attribute__((address_space(3)))
#define DEV __device__ __forceinline__
#define SB() __builtin_amdgcn_sched_barrier(0)
#define IC(x) std::integral_constant<int, (x)>{}
#define CV(x) decltype(x)::value

template <int B, int... I, class F> DEV void sfor_impl(std::integer_sequence<int, I...>, F&& f) { (f(std::integral_constant<int, B + I>{}), ...); }
template <int N, class F> DEV void sfor(F&& f) { sfor_impl<0>(std::make_integer_sequence<int, N>{}, f); }
template <int B, int E, class F> DEV void srange(F&& f) { sfor_impl<B>(std::make_integer_sequence<int, (E > B ? E - B : 0)>{}, f); }

constexpr int D = 1152, KS = 36;
constexpr unsigned O_EP = 0;
constexpr unsigned O_FL = 256;
constexpr int F_MU = 0, F_G = 192, F_H = 256, F_W = 384, F_KVC = 576, F_X = 704, F_T = 1024, F_V = 1280, F_P = 1536, F_S = 2560;
constexpr unsigned O_GATE = 524288;
constexpr unsigned O_MODP = O_GATE + 16384;
constexpr unsigned O_H = O_MODP + 81920;
constexpr unsigned O_WT = O_H + 64 * 72 * 1024;
constexpr unsigned O_T = O_WT + (16 * 324 + 8) * 1024;
constexpr unsigned O_VT = O_T + 32 * 144 * 1024;
constexpr unsigned O_P = O_VT + 16 * 288 * 1024;
constexpr unsigned O_ST = O_P + 128 * 32 * 1024;
constexpr unsigned O_KC = O_ST + 128 * 4096;
constexpr unsigned O_VC = O_KC + 360 * 1024;
constexpr unsigned O_WKT = O_VC + 360 * 1024;
constexpr unsigned O_VPT = O_WKT + 368 * 1024;
constexpr unsigned O_X2 = O_VPT + 432 * 1024;
constexpr unsigned O_SCP = O_X2 + 128 * 36 * 1024;
constexpr unsigned O_LNP = O_SCP + 128 * 40 * 1024;
constexpr unsigned O_U = O_LNP + 128 * 1024;
constexpr unsigned WS_BYTES = O_U + 2 * 18 * 80 * 4;
constexpr int LDS_MISC = 147456, LDS_BYTES = 163840;
constexpr int SC1 = 16;

#define STAMP(k, s)

DEV f32x4 mfma(v4u a, v4u b, f32x4 c) {
  return __builtin_amdgcn_mfma_f32_16x16x32_bf16(__builtin_bit_cast(bf16x8, a), __builtin_bit_cast(bf16x8, b), c, 0, 0, 0);
}
DEV unsigned pk2(float a, float b) { f32x2 v = {a, b}; return __builtin_bit_cast(unsigned, __builtin_convertvector(v, bf16x2_t)); }
DEV float asf(unsigned u) { return __builtin_bit_cast(float, u); }
DEV unsigned asu(float f) { return __builtin_bit_cast(unsigned, f); }
DEV float bflo(unsigned u) { return __builtin_bit_cast(float, u << 16); }
DEV float bfhi(unsigned u) { return __builtin_bit_cast(float, u & 0xffff0000u); }
DEV v4u pack8(f32x4 a, f32x4 b) { return v4u{pk2(a[0], a[1]), pk2(a[2], a[3]), pk2(b[0], b[1]), pk2(b[2], b[3])}; }
DEV v2u pack4(f32x4 a) { return v2u{pk2(a[0], a[1]), pk2(a[2], a[3])}; }
DEV rsrc_t mkrs(const void* p, unsigned bytes) { return __builtin_amdgcn_make_buffer_rsrc((void*)p, 0, bytes, 0x00020000); }
template <int IMM> DEV void set_m0(unsigned base) { asm volatile("s_add_u32 m0, %0, %1" ::"s"(base), "i"(IMM) : "scc"); }
template <int POL, int OFF> DEV void dma(int vo, rsrc_t rs, int so) {
  if constexpr (POL == 0) asm volatile("buffer_load_dwordx4 %0, %1, %2 offen offset:%3 lds" ::"v"(vo), "s"(rs), "s"(so), "i"(OFF) : "memory");
  else if constexpr (POL == 1) asm volatile("buffer_load_dwordx4 %0, %1, %2 offen offset:%3 nt lds" ::"v"(vo), "s"(rs), "s"(so), "i"(OFF) : "memory");
  else asm volatile("buffer_load_dwordx4 %0, %1, %2 offen offset:%3 sc1 lds" ::"v"(vo), "s"(rs), "s"(so), "i"(OFF) : "memory");
}
DEV void nop1() { asm volatile("s_nop 0"); }
template <int OFF> DEV void ald16(v4u& d, int vo, rsrc_t rs, int so) {
  asm volatile("buffer_load_dwordx4 %0, %1, %2, %3 offen offset:%4 sc1" : "=v"(d) : "v"(vo), "s"(rs), "s"(so), "i"(OFF));
}
template <int OFF> DEV void ald16p(v4u& d, int vo, rsrc_t rs, int so) {
  asm volatile("buffer_load_dwordx4 %0, %1, %2, %3 offen offset:%4" : "=v"(d) : "v"(vo), "s"(rs), "s"(so), "i"(OFF));
}
template <int OFF> DEV void ald16a(v4u& d, int vo, rsrc_t rs, int so) {
  asm volatile("buffer_load_dwordx4 %0, %1, %2, %3 offen offset:%4 sc1" : "=a"(d) : "v"(vo), "s"(rs), "s"(so), "i"(OFF));
}
template <int OFF> DEV void ald8(v2u& d, int vo, rsrc_t rs, int so) {
  asm volatile("buffer_load_dwordx2 %0, %1, %2, %3 offen offset:%4 sc1" : "=v"(d) : "v"(vo), "s"(rs), "s"(so), "i"(OFF));
}
template <int OFF> DEV void dsr16(v4u& d, unsigned addr) { asm volatile("ds_read_b128 %0, %1 offset:%2" : "=v"(d) : "v"(addr), "i"(OFF)); }
template <int OFF> DEV void dsrtr(v2u& d, unsigned addr) { asm volatile("ds_read_b64_tr_b16 %0, %1 offset:%2" : "=v"(d) : "v"(addr), "i"(OFF)); }
template <int N> DEV void waitv() { asm volatile("s_waitcnt vmcnt(%0)" ::"i"(N) : "memory"); }
template <int N> DEV void waitlv() { asm volatile("s_waitcnt vmcnt(%0) lgkmcnt(0)" ::"i"(N) : "memory"); }
template <int N, int L> DEV void waitvl() { asm volatile("s_waitcnt vmcnt(%0) lgkmcnt(%1)" ::"i"(N), "i"(L) : "memory"); }
template <int L> DEV void waitln() { asm volatile("s_waitcnt lgkmcnt(%0)" ::"i"(L) : "memory"); }
DEV void waitl() { asm volatile("s_waitcnt lgkmcnt(0)" ::: "memory"); }
DEV void bar() { asm volatile("s_barrier" ::: "memory"); }
DEV void lbar() { asm volatile("s_waitcnt lgkmcnt(0)\n\ts_barrier" ::: "memory"); }
DEV void pin(v4u& x) { asm volatile("" : "+v"(x)); }
DEV void pin(v2u& x) { asm volatile("" : "+v"(x)); }
DEV void pins(int& x) { asm volatile("" : "+s"(x)); }
DEV void pin(int& x) { asm volatile("" : "+v"(x)); }
DEV void pin(float& x) { asm volatile("" : "+v"(x)); }
DEV void pin(f32x4& x) { asm volatile("" : "+v"(x)); }
DEV void pin(f32x2& x) { asm volatile("" : "+v"(x)); }
DEV void st16(v4u v, rsrc_t rs, int vo, int so) { asm volatile("buffer_store_dwordx4 %0, %1, %2, %3 offen sc1\n\ts_nop 1" ::"v"(v), "v"(vo), "s"(rs), "s"(so) : "memory"); }
template <int OFF> DEV void st16o(v4u v, rsrc_t rs, int vo) { asm volatile("buffer_store_dwordx4 %0, %1, %2, 0 offen offset:%3\n\ts_nop 1" ::"v"(v), "v"(vo), "s"(rs), "i"(OFF) : "memory"); }
DEV float max3(float a, float b, float c) { float r; asm("v_max3_f32 %0, %1, %2, %3" : "=v"(r) : "v"(a), "v"(b), "v"(c)); return r; }
DEV float max4(f32x4 a) { return max3(max3(a[0], a[1], a[2]), a[3], a[3]); }
DEV void st8(v2u v, rsrc_t rs, int vo, int so) { __builtin_amdgcn_raw_buffer_store_b64(v, rs, vo, so, SC1); }
DEV v4u ld16(rsrc_t rs, int vo, int so) { return __builtin_amdgcn_raw_buffer_load_b128(rs, vo, so, 0); }
DEV v4u ld16h(rsrc_t rs, int vo, int so) { return __builtin_amdgcn_raw_buffer_load_b128(rs, vo, so, SC1); }
struct FMax { DEV float operator()(float a, float b) const { return max3(a, b, b); } };
struct FAdd { DEV float operator()(float a, float b) const { return a + b; } };
template <class Op> DEV float sw32(float a, float b, Op op) { asm("s_nop 1\n\tv_permlane32_swap_b32 %0, %1" : "+v"(a), "+v"(b)); return op(a, b); }
template <class Op> DEV float sw16(float a, float b, Op op) { asm("s_nop 1\n\tv_permlane16_swap_b32 %0, %1" : "+v"(a), "+v"(b)); return op(a, b); }
DEV float xmax16_32(float v) { v = sw16(v, v, FMax{}); return sw32(v, v, FMax{}); }
DEV float xsum16_32(float v) { v = sw16(v, v, FAdd{}); return sw32(v, v, FAdd{}); }
template <class Op> DEV float xred4(float v0, float v1, float v2, float v3, Op op) { return sw16(sw32(v0, v2, op), sw32(v1, v3, op), op); }
DEV void rows4(float v, float& r0, float& r1, float& r2, float& r3) {
  float a = v, b = v;
  asm("s_nop 1\n\tv_permlane16_swap_b32 %0, %1" : "+v"(a), "+v"(b));
  r0 = a; r2 = a; r1 = b; r3 = b;
  asm("s_nop 1\n\tv_permlane32_swap_b32 %0, %1" : "+v"(r0), "+v"(r2));
  asm("s_nop 1\n\tv_permlane32_swap_b32 %0, %1" : "+v"(r1), "+v"(r3));
}
DEV void touch(unsigned lb, int vo, rsrc_t rs, int so) {
  asm volatile("s_mov_b32 m0, %0\n\ts_nop 0\n\tbuffer_load_dword %1, %2, %3 offen lds" ::"s"(lb + LDS_BYTES - 256), "v"(vo), "s"(rs), "s"(so) : "memory");
}
template <bool HA, bool H_IS_A, bool FIRST> DEV void mma_h(f32x4& acc, const v4u& h, const v4u& o) {
#define MMA_(CH, X, Y) \
  if constexpr (FIRST) asm volatile("v_mfma_f32_16x16x32_bf16 %0, " X ", " Y ", 0" : "=&v"(acc) : CH(h), "v"(o)); \
  else asm volatile("v_mfma_f32_16x16x32_bf16 %0, " X ", " Y ", %0" : "+v"(acc) : CH(h), "v"(o));
  if constexpr (HA) { if constexpr (H_IS_A) { MMA_("a", "%1", "%2") } else { MMA_("a", "%2", "%1") } }
  else { if constexpr (H_IS_A) { MMA_("v", "%1", "%2") } else { MMA_("v", "%2", "%1") } }
#undef MMA_
}
#define IPF 1
template <int ID> DEV void istub() { if constexpr (IPF) asm volatile("s_branch .Lsk%c0\n.Lst%c0:\n\ts_setpc_b64 s[98:99]\n.Lsk%c0:" ::"i"(ID)); }
template <int ID> DEV void iprobe1() { asm volatile("s_call_b64 s[98:99], .Lst%c0" ::"i"(ID) : "s98", "s99"); }
template <int B, int E> DEV void iprobe(int w) { if constexpr (IPF) srange<B, E>([&](auto ic) { if (w == (CV(ic) & 3)) iprobe1<CV(ic)>(); }); }
DEV void mfma_settle() { asm volatile("s_nop 15" ::: "memory"); SB(); }

struct Ctx {
  char* ws; rsrc_t rs; unsigned lb; LAS char* lds; int lane, w; unsigned t0, t1;
  DEV void make_tag() {
    const v2u ep = *(volatile LAS v2u*)(lds + LDS_BYTES - 512 - w * 256);
    const unsigned long long t = (((unsigned long long)(unsigned)__builtin_amdgcn_readfirstlane(ep[1]) << 32) | (unsigned)__builtin_amdgcn_readfirstlane(ep[0])) + 0x9E3779B97F4A7C15ull;
    t0 = (unsigned)t; t1 = (unsigned)(t >> 32);
  }
  DEV v2u tag() const { return v2u{t0, t1}; }
};
DEV void flag_set(const Ctx& c, int f) { if (c.lane == 0) __builtin_amdgcn_raw_buffer_store_b64(c.tag(), c.rs, 0, O_FL + f * FS, SC1); }
DEV bool flag_ok(const Ctx& c, v2u v) { return __all(v[0] == c.t0 && v[1] == c.t1); }
DEV void flag_spin(const Ctx& c, int f) {
#pragma unroll 1
  for (int i = 0; i < 20000; ++i) {
    v2u v;
    asm volatile("buffer_load_dwordx2 %0, %1, %2, 0 offen offset:%3 sc1\n\ts_waitcnt vmcnt(0)" : "=&v"(v) : "v"(f * FS), "s"(c.rs), "i"(O_FL) : "memory");
    if (flag_ok(c, v)) return;
    __builtin_amdgcn_s_sleep(SLP);
  }
}
DEV void publish(const Ctx& c, int f) { waitv<0>(); bar(); if (c.w == 0) flag_set(c, f); }
DEV void publish_first(Ctx& c, int f) { waitv<0>(); c.make_tag(); bar(); if (c.w == 0) flag_set(c, f); }

template <int NM, int NR, int NO, int SPAN> struct Gaps {
  int o0[NM + 1] = {}, r0[NM + 1] = {};
  constexpr Gaps() {
    int fr[NM] = {}, nf = 0, q = 0;
    for (int m = 0; m < NM; ++m) { o0[m] = q; while (q < NO && ((2 * q + 1) * NM) / (2 * NO + !NO) == m) ++q; if (q == o0[m] && m < SPAN) fr[nf++] = m; }
    o0[NM] = NO;
    if (nf == 0) for (; nf < NM; ++nf) fr[nf] = nf;
    int r = 0;
    for (int m = 0; m < NM; ++m) { r0[m] = r; while (r < NR && fr[(r * nf) / (NR + !NR)] == m) ++r; }
    r0[NM] = NR;
  }
};
template <int NM, int NR, int NO, int SPAN = NM, class Mm, class Rd, class Prep, class Op> DEV void kstep(Mm&& mm, Rd&& rd, Prep&& prep, Op&& op) {
  constexpr Gaps<NM, NR, NO, SPAN> g{};
  srange<g.o0[0], g.o0[1]>([&](auto qc) { if constexpr (CV(qc) == g.o0[0]) prep(qc); });
  SB();
  sfor<NM>([&](auto mc) { constexpr int m = CV(mc);
    mm(mc); SB();
    srange<g.o0[m], g.o0[m + 1]>([&](auto qc) { if constexpr (CV(qc) > g.o0[m]) { prep(qc); nop1(); } op(qc); SB(); });
    srange<g.r0[m], g.r0[m + 1]>([&](auto rc) { rd(rc); SB(); });
    if constexpr (m + 1 < NM && g.o0[m + 2 > NM ? NM : m + 2] > g.o0[m + 1]) { prep(IC(g.o0[m + 1])); SB(); }
  });
}
template <int ROW, int SID, int SPLIT, int RA, int DS, int NK, int NP, int NR, int NM, class Hook, class Prep, class Issue, class Rd, class Pin, class Mm>
DEV void raw_loop(const Ctx& c, Hook&& hook, Prep&& prep, Issue&& issue, Rd&& rd, Pin&& pinf, Mm&& mm) {
  constexpr int NS = KS / NK;
  STAMP(ROW, 1);
  istub<SID>();
  if constexpr (SPLIT) {
    sfor<2>([&](auto sc) { sfor<SPLIT>([&](auto pc) { prep(sc, pc); nop1(); issue(sc, pc); }); });
    hook(IC(-1));
    sfor<2>([&](auto sc) { srange<SPLIT, NP>([&](auto pc) { prep(sc, pc); nop1(); issue(sc, pc); }); });
  } else {
    sfor<2>([&](auto sc) { sfor<NP>([&](auto pc) { prep(sc, pc); nop1(); issue(sc, pc); }); });
    hook(IC(-1));
  }
  if constexpr (DS == 3) sfor<NP>([&](auto pc) { prep(IC(2), pc); nop1(); issue(IC(2), pc); });
  SB();
  STAMP(ROW, 2);
  waitv<(DS - 1) * NP - SPLIT>(); bar();
  STAMP(ROW, 3);
  hook(IC(-2));
  sfor<RA * NR>([&](auto rc) { rd(IC(CV(rc) / NR), IC(CV(rc) % NR)); });
  SB();
  sfor<KS>([&](auto kc) {
    constexpr int k = CV(kc), s = k / NK, kk = k % NK, lg = RA == 2 && k + 1 < KS ? NR : 0;
    constexpr int p0 = (kk * NP) / NK, p1 = ((kk + 1) * NP) / NK, np = (s + DS < NS) ? p1 - p0 : 0;
    if constexpr (kk == NK - RA && s + 1 < NS) { waitvl<(s + DS < NS ? p0 : 0) + (DS == 3 && s + 2 < NS ? NP : 0), lg>(); bar(); }
    else waitln<lg>();
    if constexpr (k % 8 == 4) istub<SID + 1 + k / 8>();
    pinf(kc);
    hook(kc);
    SB();
    kstep<NM, (k + RA < KS ? NR : 0), np, (RA == 1 ? NM - NM / 4 : NM)>([&](auto mc) { mm(kc, mc); }, [&](auto rc) { rd(IC(k + RA), rc); },
      [&](auto qc) { prep(IC(s + DS), IC(p0 + CV(qc))); }, [&](auto qc) { issue(IC(s + DS), IC(p0 + CV(qc))); });
    if constexpr (k == 11) STAMP(ROW, 4);
    if constexpr (k == 23) STAMP(ROW, 5);
  });
  istub<SID + 5>();
  STAMP(ROW, 6);
}

DEV unsigned kc_rd(unsigned base, int lane, int h) { const int r = lane & 15, q = lane >> 4; return base + r * 128 + (((4 * h + q) ^ (r & 7)) << 4); }
template <bool PI> DEV int kc_src(int lane, int stride) { const int rr = lane >> 3; return (PI ? 8 * (rr >> 2) + (rr & 3) : rr) * stride + (((lane & 7) ^ rr) << 4); }
DEV int tr64_src(int lane, int par, int stride) { const int rr = lane >> 3; return rr * stride + (((lane & 7) ^ ((((rr >> 1) & 1) | ((par & 1) << 1)) << 1)) << 4); }
DEV unsigned tr64_rd(unsigned base, int lane, int w) {
  const int gg = lane >> 4, qq = (lane & 15) >> 2, pp = lane & 3;
  return base + (8 * gg + qq) * 128 + (((2 * w + (pp >> 1)) ^ (((qq >> 1) | ((gg & 1) << 1)) << 1)) << 4) + 8 * (pp & 1);
}
DEV int pi_inv_off(int t, int lane, int blk0, int blk1) {
  const int xx = 16 * (t & 1) + (lane & 15);
  return (((xx >> 2) & 1) ? blk1 : blk0) + (((lane >> 4) * 16 + 4 * (xx >> 3) + (xx & 3)) << 4);
}
DEV v4u cat(v2u a, v2u b) { return v4u{a[0], a[1], b[0], b[1]}; }
DEV v4u silu8(v4u v) {
  auto f = [](float x) { return x * __builtin_amdgcn_rcpf(1.f + __builtin_amdgcn_exp2f(x * -1.4426950408889634f)); };
  v4u o;
  for (int e = 0; e < 4; ++e) o[e] = pk2(f(bflo(v[e])), f(bfhi(v[e])));
  return o;
}

template <int TRB> DEV void w_tile(Ctx& c, const us* wqkv, const us* wo, const us* cin, const us* wmod, int ti, int tj, int pi, int ni, int pj, int nj, int mu) {
  const int lane = c.lane, w = c.w, wr = w >> 1, wc = w & 1;
  const rsrc_t ra = mkrs(wqkv, D * 3456 * 2), rb = TRB ? mkrs(wo, D * D * 2) : ra;
  const unsigned lb = c.lb;
  int va[4], vb[4];
  sfor<4>([&](auto uc) { constexpr int u = CV(uc);
    va[u] = kc_src<true>(lane, 6912) + (16 * (u & 1) + 4 * (u >> 1)) * 6912 - u * 1024;
    vb[u] = TRB ? (4 * u + (lane >> 4)) * 2304 + (((lane & 15) ^ (((lane >> 4) << 2) | u)) << 4) - u * 1024 : kc_src<false>(lane, 6912) + 8 * u * 6912 - u * 1024; });
  const int sa = (ti * 128 + 32 * w) * 6912 + (TRB ? 4608 : 0), sb = TRB ? tj * 256 + 16 * w * 2304 : (tj * 128 + 32 * w) * 6912 + 2304;
  unsigned ra_[2][2], rb_[2][8];
  sfor<2>([&](auto gc) { constexpr int g = CV(gc);
    sfor<2>([&](auto hc) { ra_[g][CV(hc)] = kc_rd(lb + g * 65536 + wr * 8192, lane, CV(hc)); });
    if constexpr (TRB) {
      const int gg = lane >> 4, qq = (lane & 15) >> 2, pp = lane & 3;
      sfor<8>([&](auto ec) { constexpr int cc = CV(ec) >> 1, t = CV(ec) & 1;
        const int row = 8 * gg + 4 * t + qq, ch = 2 * (4 * wc + cc) + (pp >> 1);
        rb_[g][CV(ec)] = lb + g * 65536 + 16384 + 256 * row + 16 * (ch ^ ((qq << 2) | ((2 * gg + t) & 3))) + 8 * (pp & 1); });
    } else sfor<2>([&](auto hc) { rb_[g][CV(hc)] = kc_rd(lb + g * 65536 + 16384 + wc * 8192, lane, CV(hc)); }); });
  const int so0 = STAG == 5 ? ((pi + pj) * 18 / 5) % 18 : STAG == 0 ? 0 : ((pi + pj) % STAG) * (18 / (STAG ? STAG : 1));
  auto stg = [&](int st) { const int t = st + so0; return t >= 18 ? t - 18 : t; };
  int eo[4][2];
  const int strip = mu >> 2, kq = mu & 3, dead = mu < 144 ? 0 : 0x7ff00000;
  v4u cv;
  ald16p<0>(cv, (lane < 36 && w < 2 ? lane * 16 : 0x7ff00000) | dead, mkrs(cin, 2 * D * 2), (w & 1) * 2304 + kq * 576);
  { const rsrc_t rw = mkrs(wmod, D * 6912 * 2);
    const int v64 = tr64_src(lane, w, 13824) | dead, s64 = strip * 128 + (kq * 288 + 8 * w) * 13824;
    sfor<9>([&](auto pc) { set_m0<98304 + CV(pc) * 4096>(lb + w * 1024); nop1(); dma<1, 0>(v64, rw, s64 + CV(pc) * 32 * 13824); }); }
  sfor<4>([&](auto jc) { constexpr int j = CV(jc);
    const int xx = 16 * (j & 1) + (lane & 15), T = tj * 8 + wc * 4 + (j & 2) + ((xx >> 2) & 1), lo = TRB ? 60 : 48;
    const bool hi = T >= lo;
    const int n = hi ? (TRB ? 3 : 6) : (TRB ? 5 : 4), U = hi ? T - lo : T, sl = (U * (hi ? (TRB ? 86 : 43) : (TRB ? 52 : 64))) >> 8;
    eo[j][0] = ((sl + (hi ? 12 : 0)) * 324 + (TRB ? 324 - 36 * n : 0) + (ti * 4 + wr * 2) * n + U - sl * n) * 1024 + (((lane >> 4) * 16 + 4 * (xx >> 3) + (xx & 3)) << 4);
    eo[j][1] = eo[j][0] + n * 1024; pin(eo[j][0]); pin(eo[j][1]); });
  iprobe<TRB * 8, TRB * 8 + 6>(w);
  { waitv<9>();
    pin(cv);
    LAS v4u* sc = (LAS v4u*)(c.lds + LDS_MISC);
    if (w < 2) { if (lane < 36) sc[w * 36 + lane] = silu8(cv); } else if (lane == 0) sc[72] = v4u{0, 0, 0, 0};
    c.make_tag();
    waitlv<0>(); bar();
    const int r = lane & 15, q = lane >> 4;
    const unsigned cb = lb + LDS_MISC + (r < 2 ? r * 576 + q * 16 : 1152), cs = r < 2 ? 64 : 0, ra2 = tr64_rd(lb + 98304, lane, w);
    v2u mt[9][2]; v4u cf[9];
    sfor<9>([&](auto ic) { constexpr int i = CV(ic); dsrtr<i * 4096>(mt[i][0], ra2); dsrtr<i * 4096 + 512>(mt[i][1], ra2); dsr16<0>(cf[i], cb + i * cs); });
    waitl();
    f32x4 m = {0, 0, 0, 0};
    sfor<9>([&](auto ic) { constexpr int i = CV(ic); pin(mt[i][0]); pin(mt[i][1]); pin(cf[i]); m = mfma(cat(mt[i][0], mt[i][1]), cf[i], m); });
    pin(m); mfma_settle();
    st16(__builtin_bit_cast(v4u, m), c.rs, (r < 2 ? r * 36864 + q * 16 : 0x7ff00000) | dead, O_MODP + kq * 9216 + strip * 256 + w * 64);
  }
  v4u fa[3][4], fb[3][4];
  v2u ft[3][8];
  f32x4 acc[4][4];
  const unsigned mw = lb + w * 4096;
  int so;
  raw_loop<0, TRB * 8, 0, 1, 3, 2, 8, TRB ? 12 : 8, 16>(c,
    [&](auto kc) { if constexpr (CV(kc) == -1) { waitv<16>(); bar(); if (w == 0 && mu < 144) flag_set(c, F_MU + mu); } },
    [&](auto sc, auto pc) { constexpr int s = CV(sc), p = CV(pc);
      if constexpr ((p & 3) == 0) { so = p == 0 ? sa + stg(s) * 128 : sb + stg(s) * (TRB ? 64 * 2304 : 128); pins(so); set_m0<(s & 3) * 32768 + (p >> 2) * 16384>(mw); } },
    [&](auto, auto pc) { constexpr int p = CV(pc); if constexpr (p < 4) dma<0, p * 1024>(va[p], ra, so); else dma<0, (p & 3) * 1024>(vb[p & 3], rb, so); },
    [&](auto kc, auto rc) { constexpr int k = CV(kc), r = CV(rc), s = (k >> 1) & 3, h = k & 1;
      if constexpr (r < 4) dsr16<(s & 1) * 32768 + r * 2048>(fa[k % 3][r], ra_[s >> 1][h]);
      else if constexpr (TRB) dsrtr<(s & 1) * 32768 + h * 8192>(ft[k % 3][r - 4], rb_[s >> 1][r - 4]);
      else dsr16<(s & 1) * 32768 + (r - 4) * 2048>(fb[k % 3][r - 4], rb_[s >> 1][h]); },
    [&](auto kc) { constexpr int k = CV(kc);
      sfor<4>([&](auto ic) { pin(fa[k % 3][CV(ic)]); });
      if constexpr (TRB) sfor<4>([&](auto ic) { constexpr int i = CV(ic); pin(ft[k % 3][2 * i]); pin(ft[k % 3][2 * i + 1]); fb[k % 3][i] = cat(ft[k % 3][2 * i], ft[k % 3][2 * i + 1]); });
      else sfor<4>([&](auto ic) { pin(fb[k % 3][CV(ic)]); }); },
    [&](auto kc, auto mc) { constexpr int k = CV(kc), i = CV(mc) & 3, j = CV(mc) >> 2;
      acc[i][j] = mfma(fa[k % 3][i], fb[k % 3][j], k == 0 ? f32x4{0, 0, 0, 0} : acc[i][j]); });
  sfor<4>([&](auto jc) { constexpr int j = CV(jc);
    sfor<2>([&](auto vc) { constexpr int v = CV(vc); st16(pack8(acc[2 * v][j], acc[2 * v + 1][j]), c.rs, eo[j][v], O_WT); }); });
  STAMP(0, 7);
  publish(c, F_W + (TRB * 9 + tj) * 9 + ti);
  STAMP(0, 8);
}

template <int KIND, class Hook> DEV void small_tile(Ctx& c, const us* wgt, const us* y, int b, int t, int rot, Hook&& hook) {
  constexpr bool TR = KIND != 0;
  constexpr int LD = KIND == 2 ? 4608 : D * 2;
  const int lane = c.lane, w = c.w;
  const rsrc_t r64 = mkrs(wgt, D * LD), r80 = KIND == 2 ? mkrs(y, 154 * D * 2) : c.rs;
  int v64[4], v80[5];
  sfor<4>([&](auto uc) { constexpr int u = CV(uc);
    v64[u] = TR ? tr64_src(lane, u, LD) + 8 * u * LD - u * 1024 : kc_src<true>(lane, LD) + (16 * (u & 1) + 4 * (u >> 1)) * LD - u * 1024; });
  sfor<5>([&](auto uc) { constexpr int u = CV(uc);
    v80[u] = KIND == 2 ? kc_src<false>(lane, D * 2) + 8 * u * (D * 2) - (u & 3) * 1024 : lane * 16 + (u >> 2) * 4096; });
  if (KIND == 2 && (lane >> 3) >= 5 && (w & 1)) v80[4] = 0x7ff00000;
  const int s64 = TR ? t * 128 + 32 * w * LD : (t * 64 + 32 * (w & 1)) * LD + (w >> 1) * 128;
  const int s80 = KIND == 2 ? (b * 77 + (w & 1) * 40) * (D * 2) + (w >> 1) * 128 : (KIND ? O_VC : O_KC) + b * (180 * 1024) + w * 5120;
  auto stg = [&](int st) { const int u = st + rot; return u >= 9 ? u - 9 : u; };
  unsigned r64a[3][2], r80a[3][2];
  sfor<3>([&](auto gc) { constexpr int g = CV(gc); const unsigned base = c.lb + g * 49152;
    if constexpr (TR) r64a[g][0] = tr64_rd(base, lane, w); else sfor<2>([&](auto hc) { r64a[g][CV(hc)] = kc_rd(base + w * 2048, lane, CV(hc)); });
    if constexpr (KIND == 2) sfor<2>([&](auto hc) { r80a[g][CV(hc)] = kc_rd(base, lane, CV(hc)); }); else r80a[g][0] = base + lane * 16; });
  v4u f64[3], f80[3][5];
  v2u ft[3][2];
  f32x4 acc[5];
  const unsigned m64 = c.lb + w * 4096, m80 = c.lb + 16384 + w * 5120;
  int so;
  raw_loop<KIND == 2 ? 2 : 4, 16 + 8 * KIND, KIND == 2 ? 0 : 4, 2, 2, 4, 9, TR ? 7 : 6, 5>(c, hook,
    [&](auto sc, auto pc) { constexpr int s = CV(sc), p = CV(pc);
      if constexpr (p == 0) { so = s64 + stg(s) * (TR ? 128 * LD : 256); pins(so); set_m0<(s & 3) * 36864>(m64); }
      if constexpr (p == 4) { so = s80 + stg(s) * (KIND == 2 ? 256 : 20480); pins(so); set_m0<(s & 3) * 36864>(m80); }
      if constexpr (p == 8) set_m0<(s & 3) * 36864 + 4096>(m80); },
    [&](auto, auto pc) { constexpr int p = CV(pc);
      if constexpr (p < 4) dma<0, p * 1024>(v64[p], r64, so); else dma<KIND == 2 ? 0 : 2, (p & 3) * 1024>(v80[p - 4], r80, so); },
    [&](auto kc, auto rc) { constexpr int k = CV(kc), r = CV(rc), kk = k & 3, so_ = ((k >> 2) & 3) * 36864;
      if constexpr (r < 5) { constexpr int o = so_ + 16384 + (KIND == 2 ? (kk >> 1) * 10240 + r * 2048 : (kk * 5 + r) * 1024); dsr16<o % 49152>(f80[k % 3][r], r80a[o / 49152][KIND == 2 ? kk & 1 : 0]); }
      else if constexpr (TR) { constexpr int o = so_ + kk * 4096 + (r - 5) * 512; dsrtr<o % 49152>(ft[k % 3][r - 5], r64a[o / 49152][0]); }
      else { constexpr int o = so_ + (kk >> 1) * 8192; dsr16<o % 49152>(f64[k % 3], r64a[o / 49152][kk & 1]); } },
    [&](auto kc) { constexpr int k = CV(kc);
      sfor<5>([&](auto ic) { pin(f80[k % 3][CV(ic)]); });
      if constexpr (TR) { pin(ft[k % 3][0]); pin(ft[k % 3][1]); f64[k % 3] = cat(ft[k % 3][0], ft[k % 3][1]); } else pin(f64[k % 3]); },
    [&](auto kc, auto mc) { constexpr int k = CV(kc), m = CV(mc);
      const f32x4 z = k == 0 ? f32x4{0, 0, 0, 0} : acc[m];
      acc[m] = KIND == 1 ? mfma(f80[k % 3][m], f64[k % 3], z) : mfma(f64[k % 3], f80[k % 3][m], z); });
  if constexpr (KIND == 2) {
    const int isv = t >= 18, n0 = (t - 18 * isv) * 64 + 16 * w, q = lane >> 4;
    const int vo = ((2 * (w & 1) + (q >> 1)) * 16 + (lane & 15)) * 16 + (q & 1) * 8;
    sfor<5>([&](auto tc) { st8(pack4(acc[CV(tc)]), c.rs, vo, (isv ? O_VC : O_KC) + ((b * 36 + (n0 >> 5)) * 5 + CV(tc)) * 1024); });
  } else if constexpr (KIND == 1) {
    const int T0 = b * 72 + t * 4 + (w & 2);
    const int off = pi_inv_off(w, lane, T0 * 3072, (T0 + 1) * 3072);
    st16(pack8(acc[0], acc[1]), c.rs, off, O_VPT);
    st16(pack8(acc[2], acc[3]), c.rs, off, O_VPT + 1024);
    st16(pack8(acc[4], f32x4{0, 0, 0, 0}), c.rs, off, O_VPT + 2048);
    publish(c, F_X + (2 + b) * 18 + t);
  } else {
    LAS float* red = (LAS float*)(c.lds + LDS_MISC);
    sfor<5>([&](auto tc) { v2u v = pack4(acc[CV(tc)]);
      if constexpr (CV(tc) == 4) { if ((lane & 15) == 13) v = v2u{0x3f803f80u, 0x3f803f80u}; }
      st8(v, c.rs, lane * 16 + (w & 1) * 8, O_WKT + ((b * 36 + 2 * t + (w >> 1)) * 5 + CV(tc)) * 1024);
      const float s = xsum16_32((bflo(v[0]) + bfhi(v[0])) + (bflo(v[1]) + bfhi(v[1])));
      if (lane < 16) red[w * 80 + CV(tc) * 16 + lane] = s; });
    lbar();
    const int tid = w * 64 + lane;
    if (tid < 80) __builtin_amdgcn_raw_buffer_store_b32(asu((red[tid] + red[80 + tid]) + (red[160 + tid] + red[240 + tid])), c.rs, tid * 4, O_U + (b * 18 + t) * 320, SC1);
    publish(c, F_X + b * 18 + t);
  }
}

DEV void gate_strip(Ctx& c, const us* cin, const us* wmod, int strip) {
  const int lane = c.lane, w = c.w, r = lane & 15, q = lane >> 4;
  const rsrc_t rw = mkrs(wmod, D * 6912 * 2), rc = mkrs(cin, 2 * D * 2);
  v4u cv, cv2;
  ald16p<0>(cv, lane * 16, rc, w * 1152);
  ald16p<1024>(cv2, lane < 8 ? lane * 16 : 0x7ff00000, rc, w * 1152);
  const int v64 = tr64_src(lane, w, 13824), s64 = 4608 + strip * 128 + 8 * w * 13824;
  sfor<36>([&](auto pc) { set_m0<CV(pc) * 4096>(c.lb + w * 1024); nop1(); dma<1, 0>(v64, rw, s64 + CV(pc) * 32 * 13824); });
  waitv<36>();
  pin(cv); pin(cv2);
  LAS v4u* sc = (LAS v4u*)(c.lds + LDS_MISC);
  sc[w * 72 + lane] = silu8(cv);
  if (lane < 8) sc[w * 72 + 64 + lane] = silu8(cv2);
  if (lane == 0) sc[288] = v4u{0, 0, 0, 0};
  const unsigned cb = c.lb + LDS_MISC + (r < 2 ? r * 2304 + q * 16 : 4608), cs = r < 2 ? 64 : 0;
  f32x4 acc = {0, 0, 0, 0};
  sfor<3>([&](auto gc) { constexpr int g = CV(gc);
    waitlv<24 - 12 * g>(); bar();
    const unsigned ra = tr64_rd(c.lb + g * 49152, lane, w);
    v2u ft[12][2]; v4u cf[12];
    sfor<12>([&](auto kc) { constexpr int k = CV(kc); dsrtr<k * 4096>(ft[k][0], ra); dsrtr<k * 4096 + 512>(ft[k][1], ra); dsr16<0>(cf[k], cb + (12 * g + k) * cs); });
    waitl();
    sfor<12>([&](auto kc) { constexpr int k = CV(kc); pin(ft[k][0]); pin(ft[k][1]); pin(cf[k]); acc = mfma(cat(ft[k][0], ft[k][1]), cf[k], acc); }); });
  const v2u pv = pack4(acc);
  if (r < 2) st16(v4u{pv[0] << 16, pv[0] & 0xffff0000u, pv[1] << 16, pv[1] & 0xffff0000u}, c.rs, r * 4608 + q * 16, O_GATE + strip * 256 + w * 64);
  publish(c, F_G + strip);
}

template <int N> DEV void ln_load(const Ctx& c, const us* xin, int rt0, int xo) {
  const rsrc_t rx = mkrs(xin, 2048 * D * 2);
  const int v = kc_src<false>(c.lane, D * 2), so = (rt0 * 16 + 8 * (c.w & 1)) * (D * 2) + (c.w >> 1) * 128;
  sfor<9 * N>([&](auto ic) { constexpr int n = CV(ic) / 9, i = CV(ic) % 9; set_m0<n * 36864 + i * 4096>(c.lb + xo + c.w * 1024); nop1(); dma<1, 0>(v, rx, so + n * (16 * D * 2) + i * 256); });
}
template <int N> DEV void ln_read(const Ctx& c, int xo, v4u (&xv)[N][9]) {
  const int w = c.w;
  const unsigned ae = kc_rd(c.lb + xo + ((9 * w) >> 1) * 2048, c.lane, w & 1), ao = kc_rd(c.lb + xo + ((9 * w + 1) >> 1) * 2048, c.lane, (w + 1) & 1);
  sfor<N>([&](auto nc) { constexpr int n = CV(nc); sfor<9>([&](auto kc) { constexpr int k = CV(kc); dsr16<(k >> 1) * 2048>(xv[n][k], ((k & 1) ? ao : ae) + n * 36864); }); });
  waitl();
  sfor<9 * N>([&](auto ic) { pin(xv[CV(ic) / 9][CV(ic) % 9]); });
}
template <int N> DEV void ln_sums(const Ctx& c, v4u (&xv)[N][9]) {
  const int r = c.lane & 15, e = r & 3;
  LAS f32x2* red = (LAS f32x2*)(c.lds + LDS_MISC + 9216);
  f32x4 s1[N], s2[N];
  sfor<9>([&](auto kc) { constexpr int k = CV(kc); sfor<N>([&](auto nc) { constexpr int n = CV(nc); const f32x4 z = {0, 0, 0, 0};
    s1[n] = mfma(v4u{0x3f803f80u, 0x3f803f80u, 0x3f803f80u, 0x3f803f80u}, xv[n][k], k ? s1[n] : z); s2[n] = mfma(xv[n][k], xv[n][k], k ? s2[n] : z); }); });
  sfor<N>([&](auto nc) { constexpr int n = CV(nc);
    const float lo = (e & 1) ? s2[n][1] : s2[n][0], hi = (e & 1) ? s2[n][3] : s2[n][2];
    if ((c.lane >> 4) == (r >> 2)) red[(n * 4 + c.w) * 16 + r] = f32x2{s1[n][0], (e & 2) ? hi : lo}; });
}
template <int N> DEV void ln_norm(const Ctx& c, v4u (&xv)[N][9], f32x2 (&nx)[N][9][4]) {
  const int r = c.lane & 15;
  LAS f32x2* red = (LAS f32x2*)(c.lds + LDS_MISC + 9216);
  sfor<N>([&](auto nc) { constexpr int n = CV(nc);
    const f32x2 s = (red[n * 64 + r] + red[n * 64 + 16 + r]) + (red[n * 64 + 32 + r] + red[n * 64 + 48 + r]);
    const float mean = s[0] * (1.f / D), rs = __builtin_amdgcn_rsqf(fmaxf(s[1] * (1.f / D) - mean * mean, 0.f) + 1e-5f), nm = -mean * rs;
    sfor<9>([&](auto kc) { constexpr int k = CV(kc); for (int e = 0; e < 4; ++e) { nx[n][k][e] = f32x2{bflo(xv[n][k][e]), bfhi(xv[n][k][e])} * rs + nm; pin(nx[n][k][e]); } }); });
}
DEV void mu_poll(const Ctx& c, v2u (&v)[3]) { sfor<3>([&](auto ic) { ald8<O_FL>(v[CV(ic)], (F_MU + CV(ic) * 48 + (c.lane < 48 ? c.lane : 0)) * FS, c.rs, 0); }); }
DEV bool mu_ok(const Ctx& c, v2u (&v)[3]) { pin(v[0]); pin(v[1]); pin(v[2]); return flag_ok(c, v[0]) && flag_ok(c, v[1]) && flag_ok(c, v[2]); }
DEV void mu_spin(const Ctx& c) {
#pragma unroll 1
  for (int i = 0; i < 20000; ++i) { v2u v[3]; mu_poll(c, v); waitv<0>(); if (mu_ok(c, v)) return; __builtin_amdgcn_s_sleep(SLP); }
}
DEV void mod_load(const Ctx& c, int b, v4u (&pv)[3][4]) {
  const int lane = c.lane, mb = O_MODP + b * 36864 + c.w * 1152;
  sfor<4>([&](auto uc) { constexpr int u = CV(uc);
    ald16<0>(pv[0][u], lane * 16, c.rs, mb + u * 9216); ald16<0>(pv[1][u], lane * 16, c.rs, mb + u * 9216 + 4608);
    ald16<1024>(pv[2][u], lane < 16 ? (lane & 7) * 16 + (lane >> 3) * 4608 : 0x7ff00000, c.rs, mb + u * 9216); });
}
template <int N> DEV void ln_apply(const Ctx& c, int rt0, v4u (&pv)[3][4], f32x2 (&nx)[N][9][4]) {
  const int lane = c.lane, w = c.w;
  LAS f32x4* ms = (LAS f32x4*)(c.lds + LDS_MISC + w * 2304);
  sfor<3>([&](auto ic) { constexpr int i = CV(ic);
    sfor<4>([&](auto uc) { pin(pv[i][CV(uc)]); });
    const v2u p = pack4((__builtin_bit_cast(f32x4, pv[i][0]) + __builtin_bit_cast(f32x4, pv[i][1])) + (__builtin_bit_cast(f32x4, pv[i][2]) + __builtin_bit_cast(f32x4, pv[i][3])));
    const f32x4 v = f32x4{bflo(p[0]), bfhi(p[0]), bflo(p[1]), bfhi(p[1])} + (i == 0 ? 0.f : i == 1 ? 1.f : (float)((lane >> 3) & 1));
    if constexpr (i < 2) ms[i * 72 + lane] = v; else if (lane < 16) ms[(lane >> 3) * 72 + 64 + (lane & 7)] = v; });
  waitl();
  const unsigned ma = c.lb + LDS_MISC + w * 2304 + (lane >> 4) * 32;
  v4u mv[3][4];
  auto rdm = [&](auto kc) { sfor<4>([&](auto ic) { dsr16<(CV(ic) >> 1) * 1152 + (CV(ic) & 1) * 16 + CV(kc) * 128>(mv[CV(kc) % 3][CV(ic)], ma); }); };
  rdm(IC(0)); rdm(IC(1));
  const int hb = O_H + 9 * w * 2048;
  sfor<9>([&](auto kc) { constexpr int k = CV(kc);
    waitln<(k < 8 ? 4 : 0)>();
    sfor<4>([&](auto ic) { pin(mv[k % 3][CV(ic)]); });
    if constexpr (k + 2 < 9) rdm(IC(k + 2));
    sfor<N>([&](auto nc) { constexpr int n = CV(nc);
      f32x2 v[4];
      sfor<4>([&](auto ec) { constexpr int e = CV(ec); const v4u t = mv[k % 3][e >> 1], a = mv[k % 3][2 + (e >> 1)];
        v[e] = nx[n][k][e] * f32x2{asf(a[2 * (e & 1)]), asf(a[2 * (e & 1) + 1])} + f32x2{asf(t[2 * (e & 1)]), asf(t[2 * (e & 1) + 1])}; pin(v[e]); });
      st16(v4u{pk2(v[0][0], v[0][1]), pk2(v[1][0], v[1][1]), pk2(v[2][0], v[2][1]), pk2(v[3][0], v[3][1])}, c.rs, lane * 16 + k * 2048, hb + ((rt0 + n) >> 1) * (72 * 1024) + ((rt0 + n) & 1) * 1024); });
    SB(); });
}

enum { OP_OCT, OP_OCT2, OP_A, OP_POLL, OP_ST };
struct Plan {
  static constexpr int MAXO = 260, MAXK = 110;
  int n = 0, kind[MAXO] = {}, idx[MAXO] = {}, wt[MAXO] = {}, need[MAXO] = {};
  int in0[MAXK] = {}, in1[MAXK] = {}, cnt[MAXK] = {}, pre = 0, cnt_pre = 0;
  bool ok = true;
  constexpr void add(int k, int i, int w, int nd) { kind[n] = k; idx[n] = i; wt[n] = w; need[n] = nd; ++n; }
  constexpr void oct(int o, int nd) { add(OP_OCT, o, 1, nd); add(OP_OCT2, o, 1, nd); }
  constexpr int left(int upto, int t) const {
    int last = -1, s = 0;
    for (int i = 0; i < upto; ++i) if (need[i] <= t) last = i;
    for (int i = last + 1; i < upto; ++i) s += wt[i];
    return s;
  }
};
template <int NT> struct P1 {
  static constexpr int NV = 9 - NT, NK = 108, NOCT = 59;
  static constexpr int H0 = 24;
  static constexpr int G_W2 = 16, G_T1 = NT == 4 ? 64 : 60, G_T2 = 80, G_V = 108, FL_T = NT == 4 ? 44 : 48, FL_V = 80;
  static constexpr int nb(int k) { return k < 36 ? NT : k < 72 ? NV : 4; }
  static constexpr int piece(int k, int j) { return k < 36 ? k * NT + j : k < 72 ? 36 * NT + (k - 36) * NV + j : 328 + (k - 72) * 4 + j; }
  static constexpr int kof(int p) { return p < 36 * NT ? p / NT : p < 324 ? 36 + (p - 36 * NT) / NV : p < 328 ? 71 : 72 + (p - 328) / 4; }
  static constexpr bool isbar(int k) { return k % 4 == 0 || k < 4 || k == 70 || k == 74; }
  static constexpr Plan make() {
    Plan p;
    auto need = [](int o) { int k = kof(8 * o) - 2; while (k >= 0 && !isbar(k)) --k; return k < 0 ? -1 : k; };
    auto gate = [](int o) { return o >= 53 ? G_T2 : o >= 41 ? G_T1 : 8 * o + 7 >= 36 * NT ? G_W2 : 0; };
    int o = 0;
    p.add(OP_ST, 2, H0, 0);
    for (; o < 4; ++o) p.oct(o, need(o));
    p.pre = p.n; p.cnt_pre = p.left(p.n, -1);
    int hf = H0;
    for (int k = 0; k < NK; ++k) {
      if (k == 36) p.add(OP_ST, 0, NT, FL_T);
      if (k == 72) p.add(OP_ST, 1, NV, FL_V);
      p.cnt[k] = p.left(p.n, k);
      p.in0[k] = p.n;
      if (k == G_W2 - 4) p.add(OP_POLL, 0, 1, G_W2);
      if (k == G_T1 - 5) p.add(OP_POLL, 1, 1, G_T1);
      if (k == G_T2 - 5) p.add(OP_POLL, 2, 1, G_T2);
      if (k == G_V - 6) p.add(OP_POLL, 3, 1, G_V);
      for (int i = 0; i < 2 && hf < 72; ++i, ++hf) p.add(OP_A, hf, 1, hf / 2);
      if (o < NOCT && gate(o) <= k && (hf == 72 || need(o) <= k + 5)) {
        bool free_ = o < 18;
        if (!free_) for (int kb = kof(8 * (o - 18) + 7); kb <= k; ++kb) if (isbar(kb)) free_ = true;
        if (free_) { if (need(o) <= k) p.ok = false; p.oct(o, need(o)); ++o; }
      }
      p.in1[k] = p.n;
      if (p.cnt[k] > 60) p.ok = false;
    }
    if (o != NOCT) p.ok = false;
    return p;
  }
};
struct P3 {
  static constexpr int NK = 32, DIST = 5, AW = 7;
  static constexpr bool isbar(int k) { return k % 2 == 1; }
  static constexpr Plan make() {
    Plan p;
    for (int k = 0; k < DIST; ++k) p.add(OP_A, k, 1, k);
    p.pre = p.n;
    int o = 18;
    for (int k = 0; k < NK; ++k) {
      p.cnt[k] = p.left(p.n, k);
      p.in0[k] = p.n;
      if (k + DIST < NK) p.add(OP_A, k + DIST, 1, k + DIST);
      if (o < 40) {
        bool free_ = false;
        for (int kb = (8 * (o - 18) + 7) / 9; kb <= k; ++kb) if (isbar(kb)) free_ = true;
        int nd = o < 36 ? (8 * o) / 9 - 2 : NK;
        while (nd < NK && !isbar(nd)) --nd;
        if (free_) { if (nd <= k) p.ok = false; p.oct(o, nd); ++o; }
      }
      p.in1[k] = p.n;
    }
    if (o != 40) p.ok = false;
    return p;
  }
};

template <int PAR> DEV void k3_epi(const Ctx& c, f32x4 (&o)[9], f32x4 (&xf)[10], v4u (&gv)[10], int nb, int qt) {
  const int lane = c.lane, vo = lane * 16, ks0 = (9 * nb) >> 1;
  istub<79 - PAR>();
  v4u wk[5][5];
  sfor<25>([&](auto ic) { dsr16<CV(ic) * 1024>(wk[CV(ic) / 5][CV(ic) % 5], c.lb + vo); });
  v4u x2[5];
  sfor<5>([&](auto uc) { constexpr int u = CV(uc);
    constexpr int tl = PAR ? 2 * u - 1 : 2 * u, th = tl + 1;
    v2u lo = {0, 0}, hi = {0, 0};
    if constexpr (tl >= 0) lo = pack4(xf[2 * u] + __builtin_bit_cast(f32x4, gv[2 * u]) * o[tl]);
    if constexpr (th <= 8) hi = pack4(xf[2 * u + 1] + __builtin_bit_cast(f32x4, gv[2 * u + 1]) * o[th]);
    x2[u] = cat(lo, hi);
    const int so = O_X2 + (qt * 36 + ks0 + u) * 1024;
    if constexpr (tl >= 0 && th <= 8) st16(x2[u], c.rs, vo, so);
    else if constexpr (tl >= 0) st8(lo, c.rs, vo, so);
    else st8(hi, c.rs, vo + 8, so); });
  waitl();
  sfor<25>([&](auto ic) { pin(wk[CV(ic) / 5][CV(ic) % 5]); });
  f32x4 s[6];
  sfor<5>([&](auto uc) { constexpr int u = CV(uc);
    sfor<5>([&](auto tc) { constexpr int t = CV(tc); s[t] = mfma(wk[u][t], x2[u], u == 0 ? f32x4{0, 0, 0, 0} : s[t]); });
    s[5] = mfma(x2[u], x2[u], u == 0 ? f32x4{0, 0, 0, 0} : s[5]); SB(); });
  sfor<6>([&](auto tc) { pin(s[CV(tc)]); });
  mfma_settle();
  sfor<5>([&](auto tc) { st16(__builtin_bit_cast(v4u, s[CV(tc)]), c.rs, vo, O_SCP + ((qt * 8 + nb) * 5 + CV(tc)) * 1024); });
  const int r = lane & 15, e = r & 3;
  if ((lane >> 4) == (r >> 2)) __builtin_amdgcn_raw_buffer_store_b32(asu(e == 0 ? s[5][0] : e == 1 ? s[5][1] : e == 2 ? s[5][2] : s[5][3]), c.rs, r * 4, O_LNP + (qt * 8 + nb) * 64, SC1);
}

template <int PAR> DEV void k4_out(v4u (&vp)[9][3], v4u (&pc)[3], v4u (&x2)[5], rsrc_t ro, int nb, int qt, int lane) {
  const int r = lane & 15, q = lane >> 4, ks0 = (9 * nb) >> 1;
  f32x4 o[9];
  sfor<9>([&](auto tc) { constexpr int t = CV(tc); o[t] = mfma(vp[t][2], pc[2], mfma(vp[t][1], pc[1], mfma(vp[t][0], pc[0], f32x4{0, 0, 0, 0}))); });
  const int oo = (qt * 16 + r) * (D * 2) + ks0 * 64 + q * 16;
  sfor<5>([&](auto uc) { constexpr int u = CV(uc); constexpr int tl = PAR ? 2 * u - 1 : 2 * u, th = tl + 1;
    v4u y = {0, 0, 0, 0};
    if constexpr (tl >= 0) { y[0] = pk2(bflo(x2[u][0]) + o[tl][0], bfhi(x2[u][0]) + o[tl][1]); y[1] = pk2(bflo(x2[u][1]) + o[tl][2], bfhi(x2[u][1]) + o[tl][3]); }
    if constexpr (th <= 8) { y[2] = pk2(bflo(x2[u][2]) + o[th][0], bfhi(x2[u][2]) + o[th][1]); y[3] = pk2(bflo(x2[u][3]) + o[th][2], bfhi(x2[u][3]) + o[th][3]); }
    if constexpr (tl >= 0 && th <= 8) st16o<u * 64>(y, ro, oo);
    else if constexpr (tl >= 0) __builtin_amdgcn_raw_buffer_store_b64(v2u{y[0], y[1]}, ro, oo, u * 64, 0);
    else __builtin_amdgcn_raw_buffer_store_b64(v2u{y[2], y[3]}, ro, oo + 8, u * 64, 0); });
}

template <int NT> DEV void tv_scores(const Ctx& c, int mi, int ci, f32x4 (&sacc)[2][4], v2u& pollv) {
  using P = P1<NT>;
  constexpr int NV = 9 - NT;
  constexpr Plan pl = P::make();
  static_assert(pl.ok, "schedule");
  const int lane = c.lane, w = c.w, vo = lane * 16, b = mi >> 3, qb = b * 16 + ci;
  v4u h[72];
  const int t0 = NT == 4 ? ci * 4 : 48 + (ci - 12) * 6, v0 = NT == 4 ? ci * 5 : 60 + (ci - 12) * 3;
  const int l9 = lane < 18 ? lane : 0;
  const int fw1 = F_W + ((l9 < 9 ? t0 : t0 + NT - 1) >> 3) * 9 + l9 % 9;
  int pf[4];
  pf[0] = F_W + 81 + ((l9 < 9 ? v0 : v0 + NV - 1) >> 3) * 9 + l9 % 9;
  pf[1] = F_T + (qb >> 1) * 16 + (lane < 12 ? lane : 0);
  pf[2] = F_T + (qb >> 1) * 16 + 12 + (lane & 3);
  { const int nb = mi & 7, s0 = nb < 7 ? (9 * nb) / 5 : 13;
    pf[3] = lane < 24 ? F_V + (b * 16 + s0 + (lane >> 3)) * 8 + (lane & 7) : lane < 42 ? F_X + b * 18 + lane - 24 : lane < 60 ? F_G + lane - 42 : F_G; }
  const int sw = O_WT + ci * (324 * 1024) + w * 2048, st = O_T + qb * (144 * 1024) + w * 2048 - 41 * 8192;
  const unsigned mw = c.lb + w * 2048;
  unsigned rd[3];
  rd[0] = c.lb + vo; rd[1] = rd[0] + 49152; rd[2] = rd[0] + 98304;
  v4u bf[3][6];
  const int rot = (mi + ci) % 3;
  int dr[3];
  sfor<3>([&](auto tc) { dr[CV(tc)] = (CV(tc) + rot) % 3 - CV(tc); });
  const int hb = O_H + (mi * 4 + w) * (72 * 1024);
  int so, sh;
  auto hprep = [&](auto fc) { constexpr int f = CV(fc); if constexpr ((f & 3) == 0) { sh = hb + (f >> 2) * 4096; if constexpr (f < 48) sh += dr[f / 16] * 16384; pins(sh); } };
  auto hload = [&](auto fc) { constexpr int f = CV(fc); if constexpr (f < 64) ald16a<(f & 3) * 1024>(h[f], vo, c.rs, sh); else ald16<(f & 3) * 1024>(h[f], vo, c.rs, sh); };
  auto prep = [&](auto oc) { constexpr int kind = pl.kind[CV(oc)], idx = pl.idx[CV(oc)];
    if constexpr (kind == OP_OCT) {
      constexpr int o0 = idx >= 41 ? 41 : 8 * idx >= 36 * NT ? 36 * NT / 8 : 0, n8 = idx >= 41 ? 4 : o0 ? NV : NT;
      so = (idx < 41 ? sw : st) + idx * 8192;
      if constexpr (idx - o0 < 3 * n8) so += dr[(idx - o0) / n8] * (n8 * 8192);
      pins(so); set_m0<((8 * idx) % 144) * 1024>(mw); }
    if constexpr (kind == OP_A) hprep(IC(idx)); };
  auto op = [&](auto oc) { constexpr int kind = pl.kind[CV(oc)], idx = pl.idx[CV(oc)];
    if constexpr (kind == OP_POLL) ald8<O_FL>(pollv, pf[idx] * FS, c.rs, 0);
    else if constexpr (kind == OP_A) hload(IC(idx));
    else if constexpr (kind != OP_ST) dma<2, (kind == OP_OCT2) * 1024>(vo, c.rs, so); };
  auto rdb = [&](auto kc, auto jc) { constexpr int p = P::piece(CV(kc), CV(jc)) % 144; dsr16<(p % 48) * 1024>(bf[CV(kc) % 3][CV(jc)], rd[p / 48]); };
  {
    bool wok = false;
#pragma unroll 1
    for (int i = 0; i < 20000; ++i) {
      v2u v;
      ald8<O_FL>(v, (lane < 18 ? fw1 : F_H + mi * 8 + 2 * w + (lane & 1)) * FS, c.rs, 0);
      waitv<0>(); pin(v);
      const unsigned long long bad = __ballot(v[0] != c.t0 || v[1] != c.t1);
      wok = !(bad & 0x3ffff);
      if (!(bad >> 18)) break;
      __builtin_amdgcn_s_sleep(SLP);
    }
    sfor<P::H0>([&](auto fc) { hprep(fc); hload(fc); });
    iprobe<(NT == 4 ? 40 : 56), (NT == 4 ? 51 : 67)>(w);
    iprobe<72, 73>(w);
    STAMP(1, 1);
    if (!wok) flag_spin(c, fw1);
  }
  STAMP(1, 2);
  sfor<pl.pre>([&](auto oc) { prep(oc); nop1(); op(oc); });
  waitv<pl.cnt_pre>(); bar();
  sfor<2 * NT>([&](auto jc) { rdb(IC(CV(jc) / NT), IC(CV(jc) % NT)); });
  SB();
  f32x4 acc[2][6];
  sfor<108>([&](auto kc) {
    constexpr int k = CV(kc), ks = k % 36, NB = P::nb(k), NM = 2 * NB;
    if constexpr (k == 36) {
      STAMP(1, 6);
      mfma_settle();
      const int tb = O_T + (mi * 2 + (w >> 1)) * (144 * 1024) + (w & 1) * 2048 + (t0 >> 1) * 4096;
      sfor<2>([&](auto ic) { sfor<NT / 2>([&](auto uc) { constexpr int i = CV(ic), u = CV(uc); st16(pack8(acc[i][2 * u], acc[i][2 * u + 1]), c.rs, vo, tb + u * 4096 + i * 1024); }); });
      asm volatile("s_nop 1"); SB();
    }
    if constexpr (k == 72) {
      STAMP(1, 10);
      mfma_settle();
      sfor<NV>([&](auto jc) { constexpr int j = CV(jc); const int T = v0 + j, nb = T / 9;
        st16(pack8(acc[0][j], acc[1][j]), c.rs, vo, O_VT + (((b * 8 + nb) * 32 + (mi & 7) * 4 + w) * 9 + T - 9 * nb) * 1024); });
      asm volatile("s_nop 1"); SB();
    }
    if constexpr (k == 0 || k == 12 || k == 24) STAMP(1, 3 + k / 12);
    if constexpr (k == 36 || k == 48 || k == 60) STAMP(1, 7 + (k - 36) / 12);
    if constexpr (k == 72 || k == 84 || k == 96) STAMP(1, 11 + (k - 72) / 12);
    if constexpr (k % 10 == 0) istub<(NT == 4 ? 40 : 56) + k / 10>();
    waitvl<pl.cnt[k], (k + 1 < 108 ? P::nb(k + 1) : 0)>();
    if constexpr (P::isbar(k)) bar();
    if constexpr (k == P::G_W2 || k == P::G_T1 || k == P::G_T2) {
      pin(pollv);
      if (!flag_ok(c, pollv)) flag_spin(c, pf[k == P::G_W2 ? 0 : k == P::G_T1 ? 1 : 2]);
    }
    if constexpr (k == P::FL_T) { if (w == 0) flag_set(c, F_T + mi * 16 + ci); }
    if constexpr (k == P::FL_V) { if (w == 0) flag_set(c, F_V + (b * 16 + ci) * 8 + (mi & 7)); }
    sfor<NB>([&](auto jc) { pin(bf[k % 3][CV(jc)]); });
    SB();
    kstep<NM, (k + 2 < 108 ? P::nb(k + 2) : 0), pl.in1[k] - pl.in0[k]>(
      [&](auto mc) { constexpr int m = CV(mc), j = m >> 1, i = m & 1, f = 2 * ks + i;
        if constexpr (k < 72) mma_h<(f < 64), (k >= 36), ks == 0>(acc[i][j], h[f], bf[k % 3][j]);
        else mma_h<(f < 64), true, ks == 0>(sacc[i][j], h[f], bf[k % 3][j]); },
      [&](auto rc) { rdb(IC(k + 2), rc); }, [&](auto qc) { prep(IC(pl.in0[k] + CV(qc))); }, [&](auto qc) { op(IC(pl.in0[k] + CV(qc))); });
  });
  STAMP(1, 14);
  mfma_settle();
  waitv<0>();
  pin(pollv);
  if (!flag_ok(c, pollv)) flag_spin(c, pf[3]);
}

__global__ __launch_bounds__(256, 1) void fused(char* ws, const us* xin, const us* cin, const us* y, const us* wqkv, const us* wo, const us* wq_c,
                                                const us* wkv, const us* wo_c, const us* wmod, us* out) {
  extern __shared__ __attribute__((aligned(16))) char smem[];
  Ctx c;
  c.ws = ws; c.rs = mkrs(ws, WS_BYTES); c.lds = (LAS char*)smem; c.lb = (unsigned)(size_t)c.lds;
  c.lane = threadIdx.x & 63; c.w = __builtin_amdgcn_readfirstlane(threadIdx.x >> 6);
  const int lane = c.lane, w = c.w, vo = lane * 16;
  STAMP(0, 0);
  asm volatile("s_mov_b32 m0, %0\n\ts_nop 0\n\tbuffer_load_dword %1, %2, 0 offen sc1 lds" ::"s"(c.lb + LDS_BYTES - 512 - w * 256), "v"(O_EP + lane * 4), "s"(c.rs) : "memory");
  const int x = blockIdx.x & 7, l = blockIdx.x >> 3;
  if constexpr (PREC) {
    unsigned long long pc;
    unsigned len;
    asm volatile("s_getpc_b64 %0\n.Lpc0:\n\ts_mov_b32 %1, .Lend0-.Lpc0" : "=s"(pc), "=s"(len));
    if (w == 3) touch(c.lb, (l * 64 + lane) * 128, mkrs((const void*)pc, len), 0);
  }
  {
    const int q = x & 3, hx = x >> 2, ni = (x & 1) ? 4 : 5, nj = (x & 2) ? 4 : 5, nw = ni * nj;
    if (l < nw) {
      const int ti = (x & 1) * 5 + l % ni, tj = ((x >> 1) & 1) * 5 + l / ni, mu = ((q == 0 ? 0 : q == 1 ? 25 : q == 2 ? 45 : 65) + l) * 2 + hx;
      if (x < 4) w_tile<0>(c, wqkv, wo, cin, wmod, ti, tj, l % ni, ni, l / ni, nj, mu); else w_tile<1>(c, wqkv, wo, cin, wmod, ti, tj, l % ni, ni, l / ni, nj, mu);
    } else {
      const int e = l - nw, nk = q == 0 ? 6 : q == 1 ? 10 : q == 2 ? 8 : 12;
      if (e < nk) {
        const int t = hx * 18 + (q == 0 ? 0 : q == 1 ? 3 : q == 2 ? 8 : 12) + (e >> 1), bb = e & 1, rot = ((e >> 1) + 5 * bb) % 9;
        const int rt = bb * 64 + 33 + t;
        const bool ln = t < 31;
        v4u xv[1][9], pv[3][4];
        f32x2 nx[1][9][4];
        v2u mp[3];
        ln_load<1>(c, xin, ln ? rt : 128, 110592);
        small_tile<2>(c, wkv, y, bb, t, rot, [&](auto kc) { constexpr int k = CV(kc);
          if constexpr (k == -1) { iprobe<33, 38>(w); if (hx) iprobe<24, 30>(w); else iprobe<16, 22>(w); }
          if constexpr (k == -2) { c.make_tag(); ln_read<1>(c, 110592, xv); ln_sums<1>(c, xv); waitl(); }
          if constexpr (k == 3) ln_norm<1>(c, xv, nx);
          if constexpr (k == 27) mu_poll(c, mp);
          if constexpr (k == 31) { if (!mu_ok(c, mp)) mu_spin(c); mod_load(c, bb, pv); } });
        STAMP(3, 1);
        waitv<5>();
        if (ln) ln_apply<1>(c, rt, pv, nx); else waitv<0>();
        STAMP(3, 2);
        waitv<9>(); bar();
        if (w == 0) flag_set(c, F_KVC + (hx * 2 + bb) * 18 + t - 18 * hx);
        STAMP(4, 0);
        auto gate = [&](auto kc) { if constexpr (CV(kc) == -1) {
          waitv<8>(); bar();
          if (w == 0 && ln) flag_set(c, F_H + rt);
          flag_spin(c, F_KVC + (hx * 2 + bb) * 18 + (lane < 18 ? lane : 0)); } };
        if (hx) small_tile<1>(c, wo_c, y, bb, t - 18, rot, gate); else small_tile<0>(c, wq_c, y, bb, t, rot, gate);
      } else {
        const int a2 = (q == 0 ? 0 : q == 1 ? 1 : q == 2 ? 3 : 7) + e - nk, rt = hx * 64 + 3 * a2;
        v4u xv[3][9], pv[3][4];
        f32x2 nx[3][9][4];
        ln_load<3>(c, xin, rt, 0);
        waitv<0>(); c.make_tag(); bar();
        ln_read<3>(c, 0, xv); ln_sums<3>(c, xv);
        lbar();
        ln_norm<3>(c, xv, nx);
        STAMP(3, 3);
        mu_spin(c);
        STAMP(3, 4);
        mod_load(c, hx, pv);
        waitv<0>();
        ln_apply<3>(c, rt, pv, nx);
        STAMP(3, 5);
        waitv<0>(); bar();
        if (w == 0 && lane < 3) __builtin_amdgcn_raw_buffer_store_b64(c.tag(), c.rs, lane * FS, O_FL + (F_H + rt) * FS, SC1);
        if (a2 < 9) gate_strip(c, cin, wmod, a2 * 2 + hx);
      }
    }
  }
  STAMP(1, 0);
  c.t0 = __builtin_amdgcn_readfirstlane(c.t0); c.t1 = __builtin_amdgcn_readfirstlane(c.t1);
  const int mi = MAP48 ? (x & 3) * 4 + (l & 3) : (x & 1) * 8 + (l & 7), ci = MAP48 ? (x >> 2) * 8 + (l >> 2) : (x >> 1) * 4 + (l >> 3), b = mi >> 3, kb = mi & 7, qb = b * 16 + ci;
  f32x4 sacc[2][4];
  v2u pollv;
  if (ci < 12) tv_scores<4>(c, mi, ci, sacc, pollv); else tv_scores<6>(c, mi, ci, sacc, pollv);
  istub<72>();
  STAMP(5, 0);
  const int nb = kb, qt = qb * 4 + w;
  const int sv = O_VT + (b * 8 + nb) * (288 * 1024) + w * 2048, rot = (ci + nb) & 3;
  auto voct = [&](int o) { const int u = o + 9 * rot; return sv + (u >= 36 ? u - 36 : u) * 8192; };
  const unsigned mw = c.lb + w * 2048;
  {
    LAS float* red = (LAS float*)(c.lds + LDS_MISC);
    const float cs = 0.029462782549439476f * 1.4426950408889634f;
    float mx[4], sm[4];
    sfor<4>([&](auto jc) { constexpr int j = CV(jc);
      mx[j] = max3(max3(sacc[0][j][0], sacc[0][j][1], sacc[0][j][2]), max3(sacc[0][j][3], sacc[1][j][0], sacc[1][j][1]), max3(sacc[1][j][2], sacc[1][j][3], sacc[1][j][3])); });
    red[lane * 4 + w] = xred4(mx[0], mx[1], mx[2], mx[3], FMax{});
    lbar();
    const f32x4 own = *(const LAS f32x4*)(red + lane * 4);
    sfor<4>([&](auto jc) { constexpr int j = CV(jc);
      const f32x4 t = *(const LAS f32x4*)(red + (j * 16 + (lane & 15)) * 4);
      const float m = max4(t) * cs;
      f32x4 p0, p1;
      for (int r = 0; r < 4; ++r) { p0[r] = __builtin_amdgcn_exp2f(sacc[0][j][r] * cs - m); p1[r] = __builtin_amdgcn_exp2f(sacc[1][j][r] * cs - m); }
      st16(pack8(p0, p1), c.rs, vo, O_P + ((qb * 4 + j) * 32 + kb * 4 + w) * 1024);
      sm[j] = ((p0[0] + p0[1]) + (p0[2] + p0[3])) + ((p1[0] + p1[1]) + (p1[2] + p1[3])); });
    st8(v2u{asu(max4(own) * cs), asu(xred4(sm[0], sm[1], sm[2], sm[3], FAdd{}))}, c.rs,
        (lane >> 4) * 4096 + (lane & 15) * 8, O_ST + qb * 16384 + kb * 512 + w * 128);
    SB();
  }
  auto vburst = [&](auto oc) { const int so = voct(CV(oc)); set_m0<CV(oc) * 8192>(mw); nop1(); dma<2, 0>(vo, c.rs, so); dma<2, 1024>(vo, c.rs, so); };
  sfor<4>(vburst);
  waitv<8>();
  flag_set(c, F_P + qb * 32 + kb * 4 + w);
  STAMP(5, 1);
  v4u xv[5], gv[10];
  { const rsrc_t rx = mkrs(xin, 2048 * D * 2);
    const int ks0 = (9 * nb) >> 1;
    sfor<5>([&](auto uc) { ald16p<CV(uc) * 64>(xv[CV(uc)], (lane & 15) * (D * 2) + (lane >> 4) * 16, rx, qt * (16 * D * 2) + ks0 * 64); });
    sfor<10>([&](auto uc) { ald16<(CV(uc) >> 1) * 128 + (CV(uc) & 1) * 16>(gv[CV(uc)], (lane >> 4) * 32, c.rs, O_GATE + b * 4608 + ks0 * 128); }); }
  srange<4, 18>(vburst);
  iprobe<73, 78>(w);
  if (nb & 1) iprobe<78, 79>(w); else iprobe<79, 80>((w + 1) & 3);
  iprobe<80, 84>(w);
  flag_spin(c, F_P + qb * 32 + (lane & 31));
  STAMP(5, 2);
  istub<73>();
  f32x4 xf[10];
  sfor<5>([&](auto uc) { constexpr int u = CV(uc); pin(xv[u]);
    xf[2 * u] = f32x4{bflo(xv[u][0]), bfhi(xv[u][0]), bflo(xv[u][1]), bfhi(xv[u][1])}; xf[2 * u + 1] = f32x4{bflo(xv[u][2]), bfhi(xv[u][2]), bflo(xv[u][3]), bfhi(xv[u][3])}; });
  sfor<10>([&](auto uc) { pin(gv[CV(uc)]); });
  f32x4 tot[9];
  {
    constexpr Plan pl = P3::make();
    static_assert(pl.ok, "schedule");
    v2u sts[8];
    sfor<8>([&](auto ic) { ald8<CV(ic) * 128>(sts[CV(ic)], (((lane >> 4) + rot) & 3) * 1024 + (lane & 15) * 8, c.rs, O_ST + qt * 4096); });
    const int sk = O_WKT + (b * 36 + ((9 * nb) >> 1)) * 5120 + w * 2048 - 36 * 8192, ab = O_P + qt * 32768;
    unsigned rd[3];
    rd[0] = c.lb + vo; rd[1] = rd[0] + 49152; rd[2] = rd[0] + 98304;
    v4u a[P3::AW], bf[3][9];
    f32x4 acc[9];
    float f[8];
    int so, sa;
    auto prep = [&](auto oc) { constexpr int kind = pl.kind[CV(oc)], idx = pl.idx[CV(oc)];
      if constexpr (kind == OP_OCT) { so = idx < 36 ? voct(idx) : sk + idx * 8192; pins(so); set_m0<((8 * idx) % 144) * 1024>(mw); }
      if constexpr (kind == OP_A && (idx & 3) == 0) { sa = ab + (((idx >> 2) + 2 * rot) & 7) * 4096; pins(sa); } };
    auto op = [&](auto oc) { constexpr int kind = pl.kind[CV(oc)], idx = pl.idx[CV(oc)];
      if constexpr (kind == OP_A) ald16<(idx & 3) * 1024>(a[idx % P3::AW], vo, c.rs, sa); else dma<2, (kind == OP_OCT2) * 1024>(vo, c.rs, so); };
    auto rdb = [&](auto kc, auto jc) { constexpr int p = (CV(kc) * 9 + CV(jc)) % 144; dsr16<(p % 48) * 1024>(bf[CV(kc) % 3][CV(jc)], rd[p / 48]); };
    sfor<pl.pre>([&](auto oc) { prep(oc); op(oc); });
    bar();
    sfor<18>([&](auto jc) { rdb(IC(CV(jc) / 9), IC(CV(jc) % 9)); });
    SB();
    sfor<32>([&](auto kc) {
      constexpr int k = CV(kc);
      if constexpr (k % 8 == 0 || k == 1) STAMP(5, k == 1 ? 4 : k == 0 ? 3 : 4 + k / 8);
      if constexpr (k % 8 == 4) istub<74 + k / 8>();
      waitvl<pl.cnt[k], (k + 1 < 32 ? 9 : 0)>();
      if constexpr (P3::isbar(k)) bar();
      sfor<9>([&](auto jc) { pin(bf[k % 3][CV(jc)]); });
      pin(a[k % P3::AW]);
      if constexpr (k == 1) {
        sfor<8>([&](auto ic) { pin(sts[CV(ic)]); });
        const float m0 = asf(sts[0][0]), m1 = asf(sts[4][0]), m = xmax16_32(max3(m0, m1, m1));
        const float e0 = __builtin_amdgcn_exp2f(m0 - m), e1 = __builtin_amdgcn_exp2f(m1 - m);
        const float s = __builtin_amdgcn_rcpf(xsum16_32(e0 * ((asf(sts[0][1]) + asf(sts[1][1])) + (asf(sts[2][1]) + asf(sts[3][1]))) + e1 * ((asf(sts[4][1]) + asf(sts[5][1])) + (asf(sts[6][1]) + asf(sts[7][1])))));
        rows4(e0 * s, f[0], f[2], f[4], f[6]); rows4(e1 * s, f[1], f[3], f[5], f[7]);
      }
      SB();
      kstep<9, (k + 2 < 32 ? 9 : 0), pl.in1[k] - pl.in0[k]>(
        [&](auto mc) { constexpr int m = CV(mc);
          if constexpr (k % 4 == 0 && k > 0) { if constexpr (k == 4) tot[m] = acc[m] * f[0]; else tot[m] += acc[m] * f[k / 4 - 1]; pin(tot[m]); }
          mma_h<false, false, k % 4 == 0>(acc[m], a[k % P3::AW], bf[k % 3][m]); },
        [&](auto rc) { rdb(IC(k + 2), rc); }, [&](auto qc) { prep(IC(pl.in0[k] + CV(qc))); }, [&](auto qc) { op(IC(pl.in0[k] + CV(qc))); });
    });
    mfma_settle();
    sfor<9>([&](auto jc) { tot[CV(jc)] += acc[CV(jc)] * f[7]; });
  }
  STAMP(5, 8);
  waitv<0>(); bar();
  if (nb & 1) k3_epi<1>(c, tot, xf, gv, nb, qt); else k3_epi<0>(c, tot, xf, gv, nb, qt);
  const int qt4 = qb * 4 + (nb >> 1), nb4 = (nb & 1) * 4 + w;
  SB();
  v4u vp[9][3];
  flag_spin(c, F_X + 36 + b * 18 + (lane < 18 ? lane : 0));
  flag_set(c, F_S + qt * 8 + nb);
  STAMP(5, 9);
  istub<80>();
  sfor<27>([&](auto ic) { constexpr int i = CV(ic); ald16<(i & 3) * 1024>(vp[i / 3][i % 3], vo, c.rs, O_VPT + (b * 72 + 9 * nb4) * 3072 + (i >> 2) * 4096); });
  LAS float* ured = (LAS float*)(c.lds + LDS_MISC + 2048);
  { const int tid = w * 64 + lane;
    if (tid < 240) { const int g = tid / 80, m = tid % 80; float u = 0.f;
      for (int i = 0; i < 6; ++i) u += asf(__builtin_amdgcn_raw_buffer_load_b32(c.rs, (g * 6 + i) * 320 + m * 4, O_U + b * 5760, SC1));
      ured[tid] = u; } }
  if (blockIdx.x == 0 && w == 3) {
    sfor<4>([&](auto ic) { flag_spin(c, F_T + CV(ic) * 64 + lane); });
    if (lane == 0) __builtin_amdgcn_raw_buffer_store_b64(c.tag(), c.rs, 0, O_EP, SC1);
  }
  flag_spin(c, F_S + qt4 * 8 + (lane & 7));
  STAMP(5, 10);
  istub<81>();
  {
    const int r = lane & 15, q = lane >> 4;
    v4u sp[2][5], x2[5];
    float lp0, lp1;
    sfor<10>([&](auto ic) { constexpr int i = CV(ic); ald16<(i & 3) * 1024>(sp[i / 5][i % 5], vo, c.rs, O_SCP + (qt4 * 8 + 2 * w) * 5120 + (i >> 2) * 4096); });
    asm volatile("buffer_load_dword %0, %2, %3, %4 offen sc1\n\tbuffer_load_dword %1, %2, %3, %4 offen offset:64 sc1" : "=&v"(lp0), "=&v"(lp1) : "v"(q * 128 + r * 4), "s"(c.rs), "s"(O_LNP + qt4 * 512));
    sfor<5>([&](auto uc) { constexpr int u = CV(uc); ald16<(u & 3) * 1024>(x2[u], vo, c.rs, O_X2 + (qt4 * 36 + ((9 * nb4) >> 1)) * 1024 + (u >> 2) * 4096); });
    waitv<5>();
    sfor<10>([&](auto ic) { pin(sp[CV(ic) / 5][CV(ic) % 5]); });
    pin(lp0); pin(lp1);
    LAS f32x4* red = (LAS f32x4*)(c.lds + 32768);
    sfor<5>([&](auto tc) { constexpr int t = CV(tc); red[(w * 5 + t) * 64 + lane] = __builtin_bit_cast(f32x4, sp[0][t]) + __builtin_bit_cast(f32x4, sp[1][t]); });
    const float sq = xsum16_32(lp0 + lp1) * (1.f / D);
    lbar();
    f32x4 s[5];
    float mx = -3.0e38f;
    sfor<5>([&](auto tc) { constexpr int t = CV(tc); s[t] = (red[t * 64 + lane] + red[(5 + t) * 64 + lane]) + (red[(10 + t) * 64 + lane] + red[(15 + t) * 64 + lane]); });
    const float mean = xsum16_32(q == 3 ? s[4][1] : 0.f) * (1.f / D);
    const float cc = 0.029462782549439476f * 1.4426950408889634f * __builtin_amdgcn_rsqf(fmaxf(sq - mean * mean, 0.f) + 1e-5f);
    sfor<5>([&](auto tc) { constexpr int t = CV(tc);
      const LAS f32x4* u4 = (const LAS f32x4*)(ured + t * 16 + q * 4);
      s[t] = (s[t] - ((u4[0] + u4[20]) + u4[40]) * mean) * cc;
      if constexpr (t == 4) for (int e = 1; e < 4; ++e) s[t][e] = q == 3 ? -3.0e38f : s[t][e];
      mx = max3(max3(mx, s[t][0], s[t][1]), s[t][2], s[t][3]); });
    istub<82>();
    mx = xmax16_32(mx);
    float sum = 0.f;
    sfor<5>([&](auto tc) { constexpr int t = CV(tc); for (int e = 0; e < 4; ++e) { s[t][e] = __builtin_amdgcn_exp2f(s[t][e] - mx); sum += s[t][e]; } });
    sum = __builtin_amdgcn_rcpf(xsum16_32(sum));
    v4u pc[3];
    pc[0] = pack8(s[0] * sum, s[1] * sum); pc[1] = pack8(s[2] * sum, s[3] * sum); pc[2] = pack8(s[4] * sum, f32x4{0, 0, 0, 0});
    waitv<0>();
    sfor<27>([&](auto ic) { pin(vp[CV(ic) / 3][CV(ic) % 3]); });
    sfor<5>([&](auto uc) { pin(x2[CV(uc)]); });
    istub<83>();
    asm volatile(".Lend0:");
    const rsrc_t ro = mkrs(out, 2048 * D * 2);
    if (w & 1) k4_out<1>(vp, pc, x2, ro, nb4, qt4, lane); else k4_out<0>(vp, pc, x2, ro, nb4, qt4, lane);
  }
}
}

extern "C" void kernel_launch(void* const* d_in, const int* in_sizes, int n_in, void* d_out, int out_size, void* d_ws, size_t ws_size, hipStream_t stream) {
  (void)hipFuncSetAttribute((const void*)fused, hipFuncAttributeMaxDynamicSharedMemorySize, LDS_BYTES);
  fused<<<256, 256, LDS_BYTES, stream>>>((char*)d_ws + (WSEND ? ((ws_size - WS_BYTES) & ~(size_t)0xfffff) : 0), (const us*)d_in[0], (const us*)d_in[1], (const us*)d_in[2], (const us*)d_in[3], (const us*)d_in[4],
                                         (const us*)d_in[5], (const us*)d_in[6], (const us*)d_in[7], (const us*)d_in[8], (us*)d_out);
}
```
